```python
import torch

def _softmax(x, axis=-1):
    return torch.softmax(x, dim=axis)
def _gelu(x):
    return torch.nn.functional.gelu(x, approximate="tanh")
def _silu(x):
    return torch.nn.functional.silu(x)
def _sigmoid(x):
    return torch.sigmoid(x)
def _layernorm(x, axis=-1, eps=1e-5):
    m = x.mean(dim=axis, keepdim=True)
    v = x.var(dim=axis, keepdim=True, unbiased=False)
    return (x - m) / torch.sqrt(v + eps)
def _scatter_add(tgt, idx, src, axis=0):
    return tgt.clone().index_add_(axis, idx, src)
def _segment_sum(src, seg, n):
    out = torch.zeros((n,) + src.shape[1:], dtype=src.dtype)
    return out.index_add_(0, seg, src)
def _pad(x, pw):
    flat = tuple(v for pair in reversed(pw) for v in pair)
    return torch.nn.functional.pad(x, flat)

kernel_name = 'gated_mix__softmax_full_mha__moe_arch_mod__prompt'

def setup_inputs():

    _w = (lambda a: (lambda t: t.to(torch.bfloat16) if t.is_floating_point() else t)(torch.as_tensor(a)))
    def _A_setup():
        B, S, D = 1, 2048, 448
        return {
            "x": _w((lambda *s: torch.randn(*s, dtype=torch.bfloat16) * 0.05)(B, S, D)),
            "wqkv": _w((lambda *s: torch.randn(*s, dtype=torch.bfloat16) * 1.6)(D, 3 * D)),
            "wo": _w((lambda *s: torch.randn(*s, dtype=torch.bfloat16) * 0.1)(D, D)),
        }
    _A_in = _A_setup()

    def _B_setup():
        B, S, D, F, C = 1, 2048, 448, 1792, 8
        return {
            "x": _w((lambda *s: torch.randn(*s, dtype=torch.bfloat16) * 0.05)(B, S, D)),
            "w_r": _w((lambda *s: torch.randn(*s, dtype=torch.bfloat16) * 3.2)(D)),
            "w_gate": _w((lambda *s: torch.randn(*s, dtype=torch.bfloat16) * 0.4)(D, F)),
            "w_up": _w((lambda *s: torch.randn(*s, dtype=torch.bfloat16) * 0.4)(D, F)),
            "w_down": _w((lambda *s: torch.randn(*s, dtype=torch.bfloat16) * 0.4)(F, D)),
        }
    _B_in = _B_setup()

    return {
        "A_x": _A_in["x"],
        "A_wqkv": _A_in["wqkv"],
        "A_wo": _A_in["wo"],
        "B_x": _B_in["x"],
        "B_w_r": _B_in["w_r"],
        "B_w_gate": _B_in["w_gate"],
        "B_w_up": _B_in["w_up"],
        "B_w_down": _B_in["w_down"],
        "gate_w": _w((lambda *s: torch.randn(*s, dtype=torch.bfloat16) * 0.05)(448) * 16.0),
    }

def reference(A_x, A_wqkv, A_wo, B_x, B_w_r, B_w_gate, B_w_up, B_w_down, gate_w):
    def _A_ref(x, wqkv, wo):
        B, S, D = x.shape
        H = 16; Dh = D // H
        qkv = (lambda x, *d: x.permute(*d))((x @ wqkv).reshape(B, S, 3, H, Dh), 2, 0, 3, 1, 4)
        q, k, v = qkv[0], qkv[1], qkv[2]
        scale = Dh ** -0.5
        scores = torch.einsum("bhqd,bhkd->bhqk", q, k) * scale
        p = _softmax(scores, axis=-1)
        o = torch.einsum("bhqk,bhkd->bhqd", p, v)
        return (lambda x, *d: x.permute(*d))(o, 0, 2, 1, 3).reshape(B, S, D) @ wo

    def _B_ref(x, w_r, w_gate, w_up, w_down):
        B, S, D = x.shape
        C = 8
        r = x @ w_r
        idx = torch.argsort(-r, axis=-1)[:, :C]
        idx_e = (lambda x, n, axis: torch.repeat_interleave(x, n, dim=axis))(idx.reshape(B, C, 1), D, axis=2)
        x_sel = (lambda x, i, axis: torch.take_along_dim(x, i, dim=axis))(x, idx_e, axis=1)
        g = _silu(x_sel @ w_gate)
        u = x_sel @ w_up
        y = (g * u) @ w_down
        r_sel = _sigmoid((lambda x, i, axis: torch.take_along_dim(x, i, dim=axis))(r, idx, axis=1))
        y = y * r_sel.reshape(B, C, 1)
        b_off = (torch.arange(B) * S).reshape(B, 1)
        flat_idx = (idx + b_off).reshape(B * C)
        zero = (x * 0.0).reshape(B * S, D)
        upd = _scatter_add(zero, flat_idx, y.reshape(B * C, D), axis=0)
        return x + upd.reshape(B, S, D)

    x = A_x
    g = _sigmoid((x * gate_w).sum(-1, keepdims=True))
    a = _A_ref(x, A_wqkv, A_wo)
    b = _B_ref(x, B_w_r, B_w_gate, B_w_up, B_w_down)
    return g * a + (1 - g) * b
```

```cpp
#include <hip/hip_runtime.h>
#include <hip/hip_bf16.h>

#define LAS __attribute__((address_space(3)))
#define GAS __attribute__((address_space(1)))
#define DI __device__ __forceinline__
typedef float f32x16 __attribute__((ext_vector_type(16)));
typedef float f32x4 __attribute__((ext_vector_type(4)));
typedef float f32x2 __attribute__((ext_vector_type(2)));
typedef __bf16 bf16x8 __attribute__((ext_vector_type(8)));
typedef __bf16 bf16x2 __attribute__((ext_vector_type(2)));
typedef short s16x4 __attribute__((ext_vector_type(4)));
typedef unsigned u32x4 __attribute__((ext_vector_type(4)));
typedef unsigned u32x2 __attribute__((ext_vector_type(2)));
typedef unsigned long long u64;
typedef __amdgpu_buffer_rsrc_t rsrc_t;

constexpr unsigned WS_EPOCH = 0, WS_KVFLAG = 4096, WS_OFLAG = 36864, WS_CAND = 69632, WS_SEL = 71680, WS_MFLAG = 73728,
                   WS_YP = 81920, WS_KV = 1u << 20, WS_O = 5u << 20, WS_DBG = 8u << 20;
constexpr unsigned L_WKV = 0, L_XR = 57344, L_WQ = 122880, L_KEY = 151552, L_QX = 155136, L_CTL = 163328;
constexpr unsigned L_WO = 0, L_MS = 114688, L_EX1 = 122880, L_EX2 = 139264;
constexpr unsigned L_OF = 114688, L_T = 147456, L_GS = 156160, L_MAP = 156288, L_YS = 156416;
constexpr unsigned LDS_BYTES = 163840;
constexpr unsigned SPIN_CAP = 1u << 17;
constexpr u64 TAG37 = (1ull << 37) - 1;

#define ATTN_ASM \
    "s_mov_b32 %[sk], 0\n" \
    "s_mov_b32 %[so], %[st]\n" \
    "buffer_load_dwordx2 v[128:129], %[vk], %[rs], %[so] offen offset:0 sc1\n" \
    "buffer_load_dwordx2 v[130:131], %[vk], %[rs], %[so] offen offset:512 sc1\n" \
    "buffer_load_dwordx2 v[132:133], %[vk], %[rs], %[so] offen offset:1024 sc1\n" \
    "buffer_load_dwordx2 v[134:135], %[vk], %[rs], %[so] offen offset:1536 sc1\n" \
    "buffer_load_dwordx4 v[136:139], %[vv], %[rs], %[so] offen offset:2048 sc1\n" \
    "buffer_load_dwordx4 v[140:143], %[vv], %[rs], %[so] offen offset:3072 sc1\n" \
    "s_add_u32 %[sk], %[sk], 0x1000\n" \
    "s_add_u32 %[so], %[st], %[sk]\n" \
    "s_and_b32 %[so], %[so], 0xffff\n" \
    "s_and_b32 %[s2], %[sk], 0x10000\n" \
    "s_or_b32 %[so], %[so], %[s2]\n" \
    "buffer_load_dwordx2 v[144:145], %[vk], %[rs], %[so] offen offset:0 sc1\n" \
    "buffer_load_dwordx2 v[146:147], %[vk], %[rs], %[so] offen offset:512 sc1\n" \
    "buffer_load_dwordx2 v[148:149], %[vk], %[rs], %[so] offen offset:1024 sc1\n" \
    "buffer_load_dwordx2 v[150:151], %[vk], %[rs], %[so] offen offset:1536 sc1\n" \
    "buffer_load_dwordx4 v[152:155], %[vv], %[rs], %[so] offen offset:2048 sc1\n" \
    "buffer_load_dwordx4 v[156:159], %[vv], %[rs], %[so] offen offset:3072 sc1\n" \
    "s_add_u32 %[sk], %[sk], 0x1000\n" \
    "s_add_u32 %[so], %[st], %[sk]\n" \
    "s_and_b32 %[so], %[so], 0xffff\n" \
    "s_and_b32 %[s2], %[sk], 0x10000\n" \
    "s_or_b32 %[so], %[so], %[s2]\n" \
    "buffer_load_dwordx2 v[160:161], %[vk], %[rs], %[so] offen offset:0 sc1\n" \
    "buffer_load_dwordx2 v[162:163], %[vk], %[rs], %[so] offen offset:512 sc1\n" \
    "buffer_load_dwordx2 v[164:165], %[vk], %[rs], %[so] offen offset:1024 sc1\n" \
    "buffer_load_dwordx2 v[166:167], %[vk], %[rs], %[so] offen offset:1536 sc1\n" \
    "buffer_load_dwordx4 v[168:171], %[vv], %[rs], %[so] offen offset:2048 sc1\n" \
    "buffer_load_dwordx4 v[172:175], %[vv], %[rs], %[so] offen offset:3072 sc1\n" \
    "s_add_u32 %[sk], %[sk], 0x1000\n" \
    "s_add_u32 %[so], %[st], %[sk]\n" \
    "s_and_b32 %[so], %[so], 0xffff\n" \
    "s_and_b32 %[s2], %[sk], 0x10000\n" \
    "s_or_b32 %[so], %[so], %[s2]\n" \
    "s_mov_b32 exec_lo, 0\n" \
    "v_mov_b32 v38, 0\n" \
    "v_mov_b32 v39, 0\n" \
    "v_mov_b32 v46, 0\n" \
    "v_mov_b32 v47, 0\n" \
    "v_mov_b32 v54, 0\n" \
    "v_mov_b32 v55, 0\n" \
    "v_mov_b32 v62, 0\n" \
    "v_mov_b32 v63, 0\n" \
    "s_mov_b32 exec_lo, -1\n" \
    "v_mov_b32 v184, 0\n" \
    "v_mov_b32 v185, 0\n" \
    "v_mov_b32 v186, 0\n" \
    "v_mov_b32 v187, 0\n" \
    "v_mov_b32 v188, 0\n" \
    "v_mov_b32 v189, 0\n" \
    "v_mov_b32 v190, 0\n" \
    "v_mov_b32 v191, 0\n" \
    "v_mov_b32 v232, 0\n" \
    "v_mov_b32 v233, 0\n" \
    "v_mov_b32 v234, 0\n" \
    "v_mov_b32 v235, 0\n" \
    "v_mov_b32 v236, 0\n" \
    "v_mov_b32 v237, 0\n" \
    "v_mov_b32 v238, 0\n" \
    "v_mov_b32 v239, 0\n" \
    "v_mov_b32 v222, 0\n" \
    "v_mov_b32 v223, 0\n" \
    "v_mov_b32 v240, 0xff800000\n" \
    "v_mov_b32 v241, 0xff800000\n" \
    "v_mov_b32 v242, 0xff800000\n" \
    "v_mov_b32 v243, 0xff800000\n" \
    "s_cmp_eq_u32 %[md], 0\n" \
    "s_cselect_b32 %[cn], 1, 16\n" \
    "s_mov_b32 %[s2], %[st]\n" \
    "s_waitcnt vmcnt(12)\n" \
    "ATT_MAXL_%=:\n" \
    "v_mfma_f32_32x32x16_bf16 v[64:79], v[128:131], v[32:35], 0\n" \
    "v_mfma_f32_32x32x16_bf16 v[64:79], v[132:135], v[36:39], v[64:79]\n" \
    "v_mfma_f32_32x32x16_bf16 v[80:95], v[128:131], v[40:43], 0\n" \
    "v_mfma_f32_32x32x16_bf16 v[80:95], v[132:135], v[44:47], v[80:95]\n" \
    "v_mfma_f32_32x32x16_bf16 v[96:111], v[128:131], v[48:51], 0\n" \
    "v_mfma_f32_32x32x16_bf16 v[96:111], v[132:135], v[52:55], v[96:111]\n" \
    "v_mfma_f32_32x32x16_bf16 v[112:127], v[128:131], v[56:59], 0\n" \
    "v_mfma_f32_32x32x16_bf16 v[112:127], v[132:135], v[60:63], v[112:127]\n" \
    "s_nop 5\n" \
    "v_max3_f32 v240, v240, v64, v65\n" \
    "v_max3_f32 v240, v240, v66, v67\n" \
    "v_max3_f32 v240, v240, v68, v69\n" \
    "v_max3_f32 v240, v240, v70, v71\n" \
    "v_max3_f32 v240, v240, v72, v73\n" \
    "v_max3_f32 v240, v240, v74, v75\n" \
    "v_max3_f32 v240, v240, v76, v77\n" \
    "v_max3_f32 v240, v240, v78, v79\n" \
    "v_max3_f32 v241, v241, v80, v81\n" \
    "v_max3_f32 v241, v241, v82, v83\n" \
    "v_max3_f32 v241, v241, v84, v85\n" \
    "v_max3_f32 v241, v241, v86, v87\n" \
    "v_max3_f32 v241, v241, v88, v89\n" \
    "v_max3_f32 v241, v241, v90, v91\n" \
    "v_max3_f32 v241, v241, v92, v93\n" \
    "v_max3_f32 v241, v241, v94, v95\n" \
    "v_max3_f32 v242, v242, v96, v97\n" \
    "v_max3_f32 v242, v242, v98, v99\n" \
    "v_max3_f32 v242, v242, v100, v101\n" \
    "v_max3_f32 v242, v242, v102, v103\n" \
    "v_max3_f32 v242, v242, v104, v105\n" \
    "v_max3_f32 v242, v242, v106, v107\n" \
    "v_max3_f32 v242, v242, v108, v109\n" \
    "v_max3_f32 v242, v242, v110, v111\n" \
    "v_max3_f32 v243, v243, v112, v113\n" \
    "v_max3_f32 v243, v243, v114, v115\n" \
    "v_max3_f32 v243, v243, v116, v117\n" \
    "v_max3_f32 v243, v243, v118, v119\n" \
    "v_max3_f32 v243, v243, v120, v121\n" \
    "v_max3_f32 v243, v243, v122, v123\n" \
    "v_max3_f32 v243, v243, v124, v125\n" \
    "v_max3_f32 v243, v243, v126, v127\n" \
    "s_sub_u32 %[cn], %[cn], 1\n" \
    "s_cmp_eq_u32 %[cn], 0\n" \
    "s_cbranch_scc1 ATT_MAXD_%=\n" \
    "s_add_u32 %[s2], %[s2], 0x1000\n" \
    "s_and_b32 %[s2], %[s2], 0xffff\n" \
    "buffer_load_dwordx2 v[128:129], %[vk], %[rs], %[s2] offen offset:0 sc1\n" \
    "buffer_load_dwordx2 v[130:131], %[vk], %[rs], %[s2] offen offset:512 sc1\n" \
    "buffer_load_dwordx2 v[132:133], %[vk], %[rs], %[s2] offen offset:1024 sc1\n" \
    "buffer_load_dwordx2 v[134:135], %[vk], %[rs], %[s2] offen offset:1536 sc1\n" \
    "s_waitcnt vmcnt(0)\n" \
    "s_branch ATT_MAXL_%=\n" \
    "ATT_MAXD_%=:\n" \
    "s_cmp_eq_u32 %[md], 0\n" \
    "s_cbranch_scc1 ATT_NOREL_%=\n" \
    "buffer_load_dwordx2 v[128:129], %[vk], %[rs], %[st] offen offset:0 sc1\n" \
    "buffer_load_dwordx2 v[130:131], %[vk], %[rs], %[st] offen offset:512 sc1\n" \
    "buffer_load_dwordx2 v[132:133], %[vk], %[rs], %[st] offen offset:1024 sc1\n" \
    "buffer_load_dwordx2 v[134:135], %[vk], %[rs], %[st] offen offset:1536 sc1\n" \
    "s_waitcnt vmcnt(0)\n" \
    "ATT_NOREL_%=:\n" \
    "v_mov_b32 v244, v240\n" \
    "v_mov_b32 v245, v241\n" \
    "v_mov_b32 v246, v242\n" \
    "v_mov_b32 v247, v243\n" \
    "v_permlane32_swap_b32 v240, v244\n" \
    "v_permlane32_swap_b32 v241, v245\n" \
    "v_permlane32_swap_b32 v242, v246\n" \
    "v_permlane32_swap_b32 v243, v247\n" \
    "v_max_f32 v240, v240, v244\n" \
    "v_max_f32 v241, v241, v245\n" \
    "v_max_f32 v242, v242, v246\n" \
    "v_max_f32 v243, v243, v247\n" \
    "s_mov_b32 exec_lo, 0\n" \
    "v_cvt_pk_bf16_f32 v192, -v240, -v240\n" \
    "v_cvt_pk_bf16_f32 v193, -v241, -v241\n" \
    "v_cvt_pk_bf16_f32 v194, -v242, -v242\n" \
    "v_cvt_pk_bf16_f32 v195, -v243, -v243\n" \
    "v_lshlrev_b32 v192, 16, v192\n" \
    "v_lshlrev_b32 v193, 16, v193\n" \
    "v_lshlrev_b32 v194, 16, v194\n" \
    "v_lshlrev_b32 v195, 16, v195\n" \
    "v_sub_f32_e64 v196, -v240, v192\n" \
    "v_sub_f32_e64 v197, -v241, v193\n" \
    "v_sub_f32_e64 v198, -v242, v194\n" \
    "v_sub_f32_e64 v199, -v243, v195\n" \
    "v_cvt_pk_bf16_f32 v38, -v240, v196\n" \
    "v_cvt_pk_bf16_f32 v46, -v241, v197\n" \
    "v_cvt_pk_bf16_f32 v54, -v242, v198\n" \
    "v_cvt_pk_bf16_f32 v62, -v243, v199\n" \
    "v_and_b32 v192, 0xffff0000, v38\n" \
    "v_and_b32 v193, 0xffff0000, v46\n" \
    "v_and_b32 v194, 0xffff0000, v54\n" \
    "v_and_b32 v195, 0xffff0000, v62\n" \
    "v_sub_f32 v196, v196, v192\n" \
    "v_sub_f32 v197, v197, v193\n" \
    "v_sub_f32 v198, v198, v194\n" \
    "v_sub_f32 v199, v199, v195\n" \
    "v_cvt_pk_bf16_f32 v39, v196, 0\n" \
    "v_cvt_pk_bf16_f32 v47, v197, 0\n" \
    "v_cvt_pk_bf16_f32 v55, v198, 0\n" \
    "v_cvt_pk_bf16_f32 v63, v199, 0\n" \
    "s_mov_b32 exec_lo, -1\n" \
    "v_mfma_f32_32x32x16_bf16 v[192:207], v[128:131], v[32:35], 0\n" \
    "v_mfma_f32_32x32x16_bf16 v[192:207], v[132:135], v[36:39], v[192:207]\n" \
    "v_mfma_f32_32x32x16_bf16 v[64:79], v[184:187], v[184:187], 0\n" \
    "v_mfma_f32_32x32x16_bf16 v[80:95], v[184:187], v[184:187], 0\n" \
    "v_mfma_f32_32x32x16_bf16 v[96:111], v[184:187], v[184:187], 0\n" \
    "v_mfma_f32_32x32x16_bf16 v[112:127], v[184:187], v[184:187], 0\n" \
    "s_mov_b32 %[cn], 4\n" \
    "s_nop 4\n" \
    "ATT_LOOP_%=:\n" \
    "v_mfma_f32_32x32x16_bf16 v[112:127], v[184:187], v[232:235], v[112:127]\n" \
    "v_cvt_pk_bf16_f32 v239, v222, v223\n" \
    "v_exp_f32 v192, v192\n" \
    "v_exp_f32 v193, v193\n" \
    "v_exp_f32 v194, v194\n" \
    "v_cvt_pk_bf16_f32 v224, v192, v193\n" \
    "v_exp_f32 v195, v195\n" \
    "v_mfma_f32_32x32x16_bf16 v[208:223], v[128:131], v[40:43], 0\n" \
    "v_cvt_pk_bf16_f32 v225, v194, v195\n" \
    "v_exp_f32 v196, v196\n" \
    "v_exp_f32 v197, v197\n" \
    "v_exp_f32 v198, v198\n" \
    "v_cvt_pk_bf16_f32 v226, v196, v197\n" \
    "v_exp_f32 v199, v199\n" \
    "v_mfma_f32_32x32x16_bf16 v[208:223], v[132:135], v[44:47], v[208:223]\n" \
    "v_cvt_pk_bf16_f32 v227, v198, v199\n" \
    "v_exp_f32 v200, v200\n" \
    "v_exp_f32 v201, v201\n" \
    "v_exp_f32 v202, v202\n" \
    "v_cvt_pk_bf16_f32 v228, v200, v201\n" \
    "v_exp_f32 v203, v203\n" \
    "v_mfma_f32_32x32x16_bf16 v[112:127], v[188:191], v[236:239], v[112:127]\n" \
    "v_cvt_pk_bf16_f32 v229, v202, v203\n" \
    "v_exp_f32 v204, v204\n" \
    "v_exp_f32 v205, v205\n" \
    "v_exp_f32 v206, v206\n" \
    "v_cvt_pk_bf16_f32 v230, v204, v205\n" \
    "v_exp_f32 v207, v207\n" \
    "v_mfma_f32_32x32x16_bf16 v[64:79], v[136:139], v[224:227], v[64:79]\n" \
    "buffer_load_dwordx2 v[176:177], %[vk], %[rs], %[so] offen offset:0 sc1\n" \
    "buffer_load_dwordx2 v[178:179], %[vk], %[rs], %[so] offen offset:512 sc1\n" \
    "buffer_load_dwordx2 v[180:181], %[vk], %[rs], %[so] offen offset:1024 sc1\n" \
    "buffer_load_dwordx2 v[182:183], %[vk], %[rs], %[so] offen offset:1536 sc1\n" \
    "buffer_load_dwordx4 v[184:187], %[vv], %[rs], %[so] offen offset:2048 sc1\n" \
    "buffer_load_dwordx4 v[188:191], %[vv], %[rs], %[so] offen offset:3072 sc1\n" \
    "v_cvt_pk_bf16_f32 v231, v206, v207\n" \
    "v_exp_f32 v208, v208\n" \
    "v_exp_f32 v209, v209\n" \
    "v_exp_f32 v210, v210\n" \
    "v_cvt_pk_bf16_f32 v232, v208, v209\n" \
    "v_exp_f32 v211, v211\n" \
    "v_mfma_f32_32x32x16_bf16 v[192:207], v[128:131], v[48:51], 0\n" \
    "s_add_u32 %[sk], %[sk], 0x1000\n" \
    "v_cvt_pk_bf16_f32 v233, v210, v211\n" \
    "v_exp_f32 v212, v212\n" \
    "v_exp_f32 v213, v213\n" \
    "v_exp_f32 v214, v214\n" \
    "v_cvt_pk_bf16_f32 v234, v212, v213\n" \
    "v_exp_f32 v215, v215\n" \
    "v_mfma_f32_32x32x16_bf16 v[192:207], v[132:135], v[52:55], v[192:207]\n" \
    "s_add_u32 %[so], %[st], %[sk]\n" \
    "v_cvt_pk_bf16_f32 v235, v214, v215\n" \
    "v_exp_f32 v216, v216\n" \
    "v_exp_f32 v217, v217\n" \
    "v_exp_f32 v218, v218\n" \
    "v_cvt_pk_bf16_f32 v236, v216, v217\n" \
    "v_exp_f32 v219, v219\n" \
    "v_mfma_f32_32x32x16_bf16 v[64:79], v[140:143], v[228:231], v[64:79]\n" \
    "s_and_b32 %[so], %[so], 0xffff\n" \
    "v_cvt_pk_bf16_f32 v237, v218, v219\n" \
    "v_exp_f32 v220, v220\n" \
    "v_exp_f32 v221, v221\n" \
    "v_exp_f32 v222, v222\n" \
    "v_cvt_pk_bf16_f32 v238, v220, v221\n" \
    "v_exp_f32 v223, v223\n" \
    "v_mfma_f32_32x32x16_bf16 v[80:95], v[136:139], v[232:235], v[80:95]\n" \
    "s_and_b32 %[s2], %[sk], 0x10000\n" \
    "v_cvt_pk_bf16_f32 v239, v222, v223\n" \
    "v_exp_f32 v192, v192\n" \
    "v_exp_f32 v193, v193\n" \
    "v_exp_f32 v194, v194\n" \
    "v_cvt_pk_bf16_f32 v224, v192, v193\n" \
    "v_exp_f32 v195, v195\n" \
    "v_mfma_f32_32x32x16_bf16 v[208:223], v[128:131], v[56:59], 0\n" \
    "s_or_b32 %[so], %[so], %[s2]\n" \
    "v_cvt_pk_bf16_f32 v225, v194, v195\n" \
    "v_exp_f32 v196, v196\n" \
    "v_exp_f32 v197, v197\n" \
    "v_exp_f32 v198, v198\n" \
    "v_cvt_pk_bf16_f32 v226, v196, v197\n" \
    "v_exp_f32 v199, v199\n" \
    "v_mfma_f32_32x32x16_bf16 v[208:223], v[132:135], v[60:63], v[208:223]\n" \
    "v_cvt_pk_bf16_f32 v227, v198, v199\n" \
    "v_exp_f32 v200, v200\n" \
    "v_exp_f32 v201, v201\n" \
    "v_exp_f32 v202, v202\n" \
    "v_cvt_pk_bf16_f32 v228, v200, v201\n" \
    "v_exp_f32 v203, v203\n" \
    "v_mfma_f32_32x32x16_bf16 v[80:95], v[140:143], v[236:239], v[80:95]\n" \
    "v_cvt_pk_bf16_f32 v229, v202, v203\n" \
    "v_exp_f32 v204, v204\n" \
    "v_exp_f32 v205, v205\n" \
    "v_exp_f32 v206, v206\n" \
    "v_cvt_pk_bf16_f32 v230, v204, v205\n" \
    "v_exp_f32 v207, v207\n" \
    "v_mfma_f32_32x32x16_bf16 v[96:111], v[136:139], v[224:227], v[96:111]\n" \
    "s_waitcnt vmcnt(12)\n" \
    "v_cvt_pk_bf16_f32 v231, v206, v207\n" \
    "v_exp_f32 v208, v208\n" \
    "v_exp_f32 v209, v209\n" \
    "v_exp_f32 v210, v210\n" \
    "v_cvt_pk_bf16_f32 v232, v208, v209\n" \
    "v_exp_f32 v211, v211\n" \
    "v_mfma_f32_32x32x16_bf16 v[192:207], v[144:147], v[32:35], 0\n" \
    "v_cvt_pk_bf16_f32 v233, v210, v211\n" \
    "v_exp_f32 v212, v212\n" \
    "v_exp_f32 v213, v213\n" \
    "v_exp_f32 v214, v214\n" \
    "v_cvt_pk_bf16_f32 v234, v212, v213\n" \
    "v_exp_f32 v215, v215\n" \
    "v_mfma_f32_32x32x16_bf16 v[192:207], v[148:151], v[36:39], v[192:207]\n" \
    "v_cvt_pk_bf16_f32 v235, v214, v215\n" \
    "v_exp_f32 v216, v216\n" \
    "v_exp_f32 v217, v217\n" \
    "v_exp_f32 v218, v218\n" \
    "v_cvt_pk_bf16_f32 v236, v216, v217\n" \
    "v_exp_f32 v219, v219\n" \
    "v_mfma_f32_32x32x16_bf16 v[96:111], v[140:143], v[228:231], v[96:111]\n" \
    "v_cvt_pk_bf16_f32 v237, v218, v219\n" \
    "v_exp_f32 v220, v220\n" \
    "v_exp_f32 v221, v221\n" \
    "v_exp_f32 v222, v222\n" \
    "v_cvt_pk_bf16_f32 v238, v220, v221\n" \
    "v_exp_f32 v223, v223\n" \
    "v_mfma_f32_32x32x16_bf16 v[112:127], v[136:139], v[232:235], v[112:127]\n" \
    "v_cvt_pk_bf16_f32 v239, v222, v223\n" \
    "v_exp_f32 v192, v192\n" \
    "v_exp_f32 v193, v193\n" \
    "v_exp_f32 v194, v194\n" \
    "v_cvt_pk_bf16_f32 v224, v192, v193\n" \
    "v_exp_f32 v195, v195\n" \
    "v_mfma_f32_32x32x16_bf16 v[208:223], v[144:147], v[40:43], 0\n" \
    "v_cvt_pk_bf16_f32 v225, v194, v195\n" \
    "v_exp_f32 v196, v196\n" \
    "v_exp_f32 v197, v197\n" \
    "v_exp_f32 v198, v198\n" \
    "v_cvt_pk_bf16_f32 v226, v196, v197\n" \
    "v_exp_f32 v199, v199\n" \
    "v_mfma_f32_32x32x16_bf16 v[208:223], v[148:151], v[44:47], v[208:223]\n" \
    "v_cvt_pk_bf16_f32 v227, v198, v199\n" \
    "v_exp_f32 v200, v200\n" \
    "v_exp_f32 v201, v201\n" \
    "v_exp_f32 v202, v202\n" \
    "v_cvt_pk_bf16_f32 v228, v200, v201\n" \
    "v_exp_f32 v203, v203\n" \
    "v_mfma_f32_32x32x16_bf16 v[112:127], v[140:143], v[236:239], v[112:127]\n" \
    "v_cvt_pk_bf16_f32 v229, v202, v203\n" \
    "v_exp_f32 v204, v204\n" \
    "v_exp_f32 v205, v205\n" \
    "v_exp_f32 v206, v206\n" \
    "v_cvt_pk_bf16_f32 v230, v204, v205\n" \
    "v_exp_f32 v207, v207\n" \
    "v_mfma_f32_32x32x16_bf16 v[64:79], v[152:155], v[224:227], v[64:79]\n" \
    "buffer_load_dwordx2 v[128:129], %[vk], %[rs], %[so] offen offset:0 sc1\n" \
    "buffer_load_dwordx2 v[130:131], %[vk], %[rs], %[so] offen offset:512 sc1\n" \
    "buffer_load_dwordx2 v[132:133], %[vk], %[rs], %[so] offen offset:1024 sc1\n" \
    "buffer_load_dwordx2 v[134:135], %[vk], %[rs], %[so] offen offset:1536 sc1\n" \
    "buffer_load_dwordx4 v[136:139], %[vv], %[rs], %[so] offen offset:2048 sc1\n" \
    "buffer_load_dwordx4 v[140:143], %[vv], %[rs], %[so] offen offset:3072 sc1\n" \
    "v_cvt_pk_bf16_f32 v231, v206, v207\n" \
    "v_exp_f32 v208, v208\n" \
    "v_exp_f32 v209, v209\n" \
    "v_exp_f32 v210, v210\n" \
    "v_cvt_pk_bf16_f32 v232, v208, v209\n" \
    "v_exp_f32 v211, v211\n" \
    "v_mfma_f32_32x32x16_bf16 v[192:207], v[144:147], v[48:51], 0\n" \
    "s_add_u32 %[sk], %[sk], 0x1000\n" \
    "v_cvt_pk_bf16_f32 v233, v210, v211\n" \
    "v_exp_f32 v212, v212\n" \
    "v_exp_f32 v213, v213\n" \
    "v_exp_f32 v214, v214\n" \
    "v_cvt_pk_bf16_f32 v234, v212, v213\n" \
    "v_exp_f32 v215, v215\n" \
    "v_mfma_f32_32x32x16_bf16 v[192:207], v[148:151], v[52:55], v[192:207]\n" \
    "s_add_u32 %[so], %[st], %[sk]\n" \
    "v_cvt_pk_bf16_f32 v235, v214, v215\n" \
    "v_exp_f32 v216, v216\n" \
    "v_exp_f32 v217, v217\n" \
    "v_exp_f32 v218, v218\n" \
    "v_cvt_pk_bf16_f32 v236, v216, v217\n" \
    "v_exp_f32 v219, v219\n" \
    "v_mfma_f32_32x32x16_bf16 v[64:79], v[156:159], v[228:231], v[64:79]\n" \
    "s_and_b32 %[so], %[so], 0xffff\n" \
    "v_cvt_pk_bf16_f32 v237, v218, v219\n" \
    "v_exp_f32 v220, v220\n" \
    "v_exp_f32 v221, v221\n" \
    "v_exp_f32 v222, v222\n" \
    "v_cvt_pk_bf16_f32 v238, v220, v221\n" \
    "v_exp_f32 v223, v223\n" \
    "v_mfma_f32_32x32x16_bf16 v[80:95], v[152:155], v[232:235], v[80:95]\n" \
    "s_and_b32 %[s2], %[sk], 0x10000\n" \
    "v_cvt_pk_bf16_f32 v239, v222, v223\n" \
    "v_exp_f32 v192, v192\n" \
    "v_exp_f32 v193, v193\n" \
    "v_exp_f32 v194, v194\n" \
    "v_cvt_pk_bf16_f32 v224, v192, v193\n" \
    "v_exp_f32 v195, v195\n" \
    "v_mfma_f32_32x32x16_bf16 v[208:223], v[144:147], v[56:59], 0\n" \
    "s_or_b32 %[so], %[so], %[s2]\n" \
    "v_cvt_pk_bf16_f32 v225, v194, v195\n" \
    "v_exp_f32 v196, v196\n" \
    "v_exp_f32 v197, v197\n" \
    "v_exp_f32 v198, v198\n" \
    "v_cvt_pk_bf16_f32 v226, v196, v197\n" \
    "v_exp_f32 v199, v199\n" \
    "v_mfma_f32_32x32x16_bf16 v[208:223], v[148:151], v[60:63], v[208:223]\n" \
    "v_cvt_pk_bf16_f32 v227, v198, v199\n" \
    "v_exp_f32 v200, v200\n" \
    "v_exp_f32 v201, v201\n" \
    "v_exp_f32 v202, v202\n" \
    "v_cvt_pk_bf16_f32 v228, v200, v201\n" \
    "v_exp_f32 v203, v203\n" \
    "v_mfma_f32_32x32x16_bf16 v[80:95], v[156:159], v[236:239], v[80:95]\n" \
    "v_cvt_pk_bf16_f32 v229, v202, v203\n" \
    "v_exp_f32 v204, v204\n" \
    "v_exp_f32 v205, v205\n" \
    "v_exp_f32 v206, v206\n" \
    "v_cvt_pk_bf16_f32 v230, v204, v205\n" \
    "v_exp_f32 v207, v207\n" \
    "v_mfma_f32_32x32x16_bf16 v[96:111], v[152:155], v[224:227], v[96:111]\n" \
    "s_waitcnt vmcnt(12)\n" \
    "v_cvt_pk_bf16_f32 v231, v206, v207\n" \
    "v_exp_f32 v208, v208\n" \
    "v_exp_f32 v209, v209\n" \
    "v_exp_f32 v210, v210\n" \
    "v_cvt_pk_bf16_f32 v232, v208, v209\n" \
    "v_exp_f32 v211, v211\n" \
    "v_mfma_f32_32x32x16_bf16 v[192:207], v[160:163], v[32:35], 0\n" \
    "v_cvt_pk_bf16_f32 v233, v210, v211\n" \
    "v_exp_f32 v212, v212\n" \
    "v_exp_f32 v213, v213\n" \
    "v_exp_f32 v214, v214\n" \
    "v_cvt_pk_bf16_f32 v234, v212, v213\n" \
    "v_exp_f32 v215, v215\n" \
    "v_mfma_f32_32x32x16_bf16 v[192:207], v[164:167], v[36:39], v[192:207]\n" \
    "v_cvt_pk_bf16_f32 v235, v214, v215\n" \
    "v_exp_f32 v216, v216\n" \
    "v_exp_f32 v217, v217\n" \
    "v_exp_f32 v218, v218\n" \
    "v_cvt_pk_bf16_f32 v236, v216, v217\n" \
    "v_exp_f32 v219, v219\n" \
    "v_mfma_f32_32x32x16_bf16 v[96:111], v[156:159], v[228:231], v[96:111]\n" \
    "v_cvt_pk_bf16_f32 v237, v218, v219\n" \
    "v_exp_f32 v220, v220\n" \
    "v_exp_f32 v221, v221\n" \
    "v_exp_f32 v222, v222\n" \
    "v_cvt_pk_bf16_f32 v238, v220, v221\n" \
    "v_exp_f32 v223, v223\n" \
    "v_mfma_f32_32x32x16_bf16 v[112:127], v[152:155], v[232:235], v[112:127]\n" \
    "v_cvt_pk_bf16_f32 v239, v222, v223\n" \
    "v_exp_f32 v192, v192\n" \
    "v_exp_f32 v193, v193\n" \
    "v_exp_f32 v194, v194\n" \
    "v_cvt_pk_bf16_f32 v224, v192, v193\n" \
    "v_exp_f32 v195, v195\n" \
    "v_mfma_f32_32x32x16_bf16 v[208:223], v[160:163], v[40:43], 0\n" \
    "v_cvt_pk_bf16_f32 v225, v194, v195\n" \
    "v_exp_f32 v196, v196\n" \
    "v_exp_f32 v197, v197\n" \
    "v_exp_f32 v198, v198\n" \
    "v_cvt_pk_bf16_f32 v226, v196, v197\n" \
    "v_exp_f32 v199, v199\n" \
    "v_mfma_f32_32x32x16_bf16 v[208:223], v[164:167], v[44:47], v[208:223]\n" \
    "v_cvt_pk_bf16_f32 v227, v198, v199\n" \
    "v_exp_f32 v200, v200\n" \
    "v_exp_f32 v201, v201\n" \
    "v_exp_f32 v202, v202\n" \
    "v_cvt_pk_bf16_f32 v228, v200, v201\n" \
    "v_exp_f32 v203, v203\n" \
    "v_mfma_f32_32x32x16_bf16 v[112:127], v[156:159], v[236:239], v[112:127]\n" \
    "v_cvt_pk_bf16_f32 v229, v202, v203\n" \
    "v_exp_f32 v204, v204\n" \
    "v_exp_f32 v205, v205\n" \
    "v_exp_f32 v206, v206\n" \
    "v_cvt_pk_bf16_f32 v230, v204, v205\n" \
    "v_exp_f32 v207, v207\n" \
    "v_mfma_f32_32x32x16_bf16 v[64:79], v[168:171], v[224:227], v[64:79]\n" \
    "buffer_load_dwordx2 v[144:145], %[vk], %[rs], %[so] offen offset:0 sc1\n" \
    "buffer_load_dwordx2 v[146:147], %[vk], %[rs], %[so] offen offset:512 sc1\n" \
    "buffer_load_dwordx2 v[148:149], %[vk], %[rs], %[so] offen offset:1024 sc1\n" \
    "buffer_load_dwordx2 v[150:151], %[vk], %[rs], %[so] offen offset:1536 sc1\n" \
    "buffer_load_dwordx4 v[152:155], %[vv], %[rs], %[so] offen offset:2048 sc1\n" \
    "buffer_load_dwordx4 v[156:159], %[vv], %[rs], %[so] offen offset:3072 sc1\n" \
    "v_cvt_pk_bf16_f32 v231, v206, v207\n" \
    "v_exp_f32 v208, v208\n" \
    "v_exp_f32 v209, v209\n" \
    "v_exp_f32 v210, v210\n" \
    "v_cvt_pk_bf16_f32 v232, v208, v209\n" \
    "v_exp_f32 v211, v211\n" \
    "v_mfma_f32_32x32x16_bf16 v[192:207], v[160:163], v[48:51], 0\n" \
    "s_add_u32 %[sk], %[sk], 0x1000\n" \
    "v_cvt_pk_bf16_f32 v233, v210, v211\n" \
    "v_exp_f32 v212, v212\n" \
    "v_exp_f32 v213, v213\n" \
    "v_exp_f32 v214, v214\n" \
    "v_cvt_pk_bf16_f32 v234, v212, v213\n" \
    "v_exp_f32 v215, v215\n" \
    "v_mfma_f32_32x32x16_bf16 v[192:207], v[164:167], v[52:55], v[192:207]\n" \
    "s_add_u32 %[so], %[st], %[sk]\n" \
    "v_cvt_pk_bf16_f32 v235, v214, v215\n" \
    "v_exp_f32 v216, v216\n" \
    "v_exp_f32 v217, v217\n" \
    "v_exp_f32 v218, v218\n" \
    "v_cvt_pk_bf16_f32 v236, v216, v217\n" \
    "v_exp_f32 v219, v219\n" \
    "v_mfma_f32_32x32x16_bf16 v[64:79], v[172:175], v[228:231], v[64:79]\n" \
    "s_and_b32 %[so], %[so], 0xffff\n" \
    "v_cvt_pk_bf16_f32 v237, v218, v219\n" \
    "v_exp_f32 v220, v220\n" \
    "v_exp_f32 v221, v221\n" \
    "v_exp_f32 v222, v222\n" \
    "v_cvt_pk_bf16_f32 v238, v220, v221\n" \
    "v_exp_f32 v223, v223\n" \
    "v_mfma_f32_32x32x16_bf16 v[80:95], v[168:171], v[232:235], v[80:95]\n" \
    "s_and_b32 %[s2], %[sk], 0x10000\n" \
    "v_cvt_pk_bf16_f32 v239, v222, v223\n" \
    "v_exp_f32 v192, v192\n" \
    "v_exp_f32 v193, v193\n" \
    "v_exp_f32 v194, v194\n" \
    "v_cvt_pk_bf16_f32 v224, v192, v193\n" \
    "v_exp_f32 v195, v195\n" \
    "v_mfma_f32_32x32x16_bf16 v[208:223], v[160:163], v[56:59], 0\n" \
    "s_or_b32 %[so], %[so], %[s2]\n" \
    "v_cvt_pk_bf16_f32 v225, v194, v195\n" \
    "v_exp_f32 v196, v196\n" \
    "v_exp_f32 v197, v197\n" \
    "v_exp_f32 v198, v198\n" \
    "v_cvt_pk_bf16_f32 v226, v196, v197\n" \
    "v_exp_f32 v199, v199\n" \
    "v_mfma_f32_32x32x16_bf16 v[208:223], v[164:167], v[60:63], v[208:223]\n" \
    "v_cvt_pk_bf16_f32 v227, v198, v199\n" \
    "v_exp_f32 v200, v200\n" \
    "v_exp_f32 v201, v201\n" \
    "v_exp_f32 v202, v202\n" \
    "v_cvt_pk_bf16_f32 v228, v200, v201\n" \
    "v_exp_f32 v203, v203\n" \
    "v_mfma_f32_32x32x16_bf16 v[80:95], v[172:175], v[236:239], v[80:95]\n" \
    "v_cvt_pk_bf16_f32 v229, v202, v203\n" \
    "v_exp_f32 v204, v204\n" \
    "v_exp_f32 v205, v205\n" \
    "v_exp_f32 v206, v206\n" \
    "v_cvt_pk_bf16_f32 v230, v204, v205\n" \
    "v_exp_f32 v207, v207\n" \
    "v_mfma_f32_32x32x16_bf16 v[96:111], v[168:171], v[224:227], v[96:111]\n" \
    "s_waitcnt vmcnt(12)\n" \
    "v_cvt_pk_bf16_f32 v231, v206, v207\n" \
    "v_exp_f32 v208, v208\n" \
    "v_exp_f32 v209, v209\n" \
    "v_exp_f32 v210, v210\n" \
    "v_cvt_pk_bf16_f32 v232, v208, v209\n" \
    "v_exp_f32 v211, v211\n" \
    "v_mfma_f32_32x32x16_bf16 v[192:207], v[176:179], v[32:35], 0\n" \
    "v_cvt_pk_bf16_f32 v233, v210, v211\n" \
    "v_exp_f32 v212, v212\n" \
    "v_exp_f32 v213, v213\n" \
    "v_exp_f32 v214, v214\n" \
    "v_cvt_pk_bf16_f32 v234, v212, v213\n" \
    "v_exp_f32 v215, v215\n" \
    "v_mfma_f32_32x32x16_bf16 v[192:207], v[180:183], v[36:39], v[192:207]\n" \
    "v_cvt_pk_bf16_f32 v235, v214, v215\n" \
    "v_exp_f32 v216, v216\n" \
    "v_exp_f32 v217, v217\n" \
    "v_exp_f32 v218, v218\n" \
    "v_cvt_pk_bf16_f32 v236, v216, v217\n" \
    "v_exp_f32 v219, v219\n" \
    "v_mfma_f32_32x32x16_bf16 v[96:111], v[172:175], v[228:231], v[96:111]\n" \
    "v_cvt_pk_bf16_f32 v237, v218, v219\n" \
    "v_exp_f32 v220, v220\n" \
    "v_exp_f32 v221, v221\n" \
    "v_exp_f32 v222, v222\n" \
    "v_cvt_pk_bf16_f32 v238, v220, v221\n" \
    "v_exp_f32 v223, v223\n" \
    "v_mfma_f32_32x32x16_bf16 v[112:127], v[168:171], v[232:235], v[112:127]\n" \
    "v_cvt_pk_bf16_f32 v239, v222, v223\n" \
    "v_exp_f32 v192, v192\n" \
    "v_exp_f32 v193, v193\n" \
    "v_exp_f32 v194, v194\n" \
    "v_cvt_pk_bf16_f32 v224, v192, v193\n" \
    "v_exp_f32 v195, v195\n" \
    "v_mfma_f32_32x32x16_bf16 v[208:223], v[176:179], v[40:43], 0\n" \
    "v_cvt_pk_bf16_f32 v225, v194, v195\n" \
    "v_exp_f32 v196, v196\n" \
    "v_exp_f32 v197, v197\n" \
    "v_exp_f32 v198, v198\n" \
    "v_cvt_pk_bf16_f32 v226, v196, v197\n" \
    "v_exp_f32 v199, v199\n" \
    "v_mfma_f32_32x32x16_bf16 v[208:223], v[180:183], v[44:47], v[208:223]\n" \
    "v_cvt_pk_bf16_f32 v227, v198, v199\n" \
    "v_exp_f32 v200, v200\n" \
    "v_exp_f32 v201, v201\n" \
    "v_exp_f32 v202, v202\n" \
    "v_cvt_pk_bf16_f32 v228, v200, v201\n" \
    "v_exp_f32 v203, v203\n" \
    "v_mfma_f32_32x32x16_bf16 v[112:127], v[172:175], v[236:239], v[112:127]\n" \
    "v_cvt_pk_bf16_f32 v229, v202, v203\n" \
    "v_exp_f32 v204, v204\n" \
    "v_exp_f32 v205, v205\n" \
    "v_exp_f32 v206, v206\n" \
    "v_cvt_pk_bf16_f32 v230, v204, v205\n" \
    "v_exp_f32 v207, v207\n" \
    "v_mfma_f32_32x32x16_bf16 v[64:79], v[184:187], v[224:227], v[64:79]\n" \
    "buffer_load_dwordx2 v[160:161], %[vk], %[rs], %[so] offen offset:0 sc1\n" \
    "buffer_load_dwordx2 v[162:163], %[vk], %[rs], %[so] offen offset:512 sc1\n" \
    "buffer_load_dwordx2 v[164:165], %[vk], %[rs], %[so] offen offset:1024 sc1\n" \
    "buffer_load_dwordx2 v[166:167], %[vk], %[rs], %[so] offen offset:1536 sc1\n" \
    "buffer_load_dwordx4 v[168:171], %[vv], %[rs], %[so] offen offset:2048 sc1\n" \
    "buffer_load_dwordx4 v[172:175], %[vv], %[rs], %[so] offen offset:3072 sc1\n" \
    "v_cvt_pk_bf16_f32 v231, v206, v207\n" \
    "v_exp_f32 v208, v208\n" \
    "v_exp_f32 v209, v209\n" \
    "v_exp_f32 v210, v210\n" \
    "v_cvt_pk_bf16_f32 v232, v208, v209\n" \
    "v_exp_f32 v211, v211\n" \
    "v_mfma_f32_32x32x16_bf16 v[192:207], v[176:179], v[48:51], 0\n" \
    "s_add_u32 %[sk], %[sk], 0x1000\n" \
    "v_cvt_pk_bf16_f32 v233, v210, v211\n" \
    "v_exp_f32 v212, v212\n" \
    "v_exp_f32 v213, v213\n" \
    "v_exp_f32 v214, v214\n" \
    "v_cvt_pk_bf16_f32 v234, v212, v213\n" \
    "v_exp_f32 v215, v215\n" \
    "v_mfma_f32_32x32x16_bf16 v[192:207], v[180:183], v[52:55], v[192:207]\n" \
    "s_add_u32 %[so], %[st], %[sk]\n" \
    "v_cvt_pk_bf16_f32 v235, v214, v215\n" \
    "v_exp_f32 v216, v216\n" \
    "v_exp_f32 v217, v217\n" \
    "v_exp_f32 v218, v218\n" \
    "v_cvt_pk_bf16_f32 v236, v216, v217\n" \
    "v_exp_f32 v219, v219\n" \
    "v_mfma_f32_32x32x16_bf16 v[64:79], v[188:191], v[228:231], v[64:79]\n" \
    "s_and_b32 %[so], %[so], 0xffff\n" \
    "v_cvt_pk_bf16_f32 v237, v218, v219\n" \
    "v_exp_f32 v220, v220\n" \
    "v_exp_f32 v221, v221\n" \
    "v_exp_f32 v222, v222\n" \
    "v_cvt_pk_bf16_f32 v238, v220, v221\n" \
    "v_exp_f32 v223, v223\n" \
    "v_mfma_f32_32x32x16_bf16 v[80:95], v[184:187], v[232:235], v[80:95]\n" \
    "s_and_b32 %[s2], %[sk], 0x10000\n" \
    "v_cvt_pk_bf16_f32 v239, v222, v223\n" \
    "v_exp_f32 v192, v192\n" \
    "v_exp_f32 v193, v193\n" \
    "v_exp_f32 v194, v194\n" \
    "v_cvt_pk_bf16_f32 v224, v192, v193\n" \
    "v_exp_f32 v195, v195\n" \
    "v_mfma_f32_32x32x16_bf16 v[208:223], v[176:179], v[56:59], 0\n" \
    "s_or_b32 %[so], %[so], %[s2]\n" \
    "v_cvt_pk_bf16_f32 v225, v194, v195\n" \
    "v_exp_f32 v196, v196\n" \
    "v_exp_f32 v197, v197\n" \
    "v_exp_f32 v198, v198\n" \
    "v_cvt_pk_bf16_f32 v226, v196, v197\n" \
    "v_exp_f32 v199, v199\n" \
    "v_mfma_f32_32x32x16_bf16 v[208:223], v[180:183], v[60:63], v[208:223]\n" \
    "v_cvt_pk_bf16_f32 v227, v198, v199\n" \
    "v_exp_f32 v200, v200\n" \
    "v_exp_f32 v201, v201\n" \
    "v_exp_f32 v202, v202\n" \
    "v_cvt_pk_bf16_f32 v228, v200, v201\n" \
    "v_exp_f32 v203, v203\n" \
    "v_mfma_f32_32x32x16_bf16 v[80:95], v[188:191], v[236:239], v[80:95]\n" \
    "v_cvt_pk_bf16_f32 v229, v202, v203\n" \
    "v_exp_f32 v204, v204\n" \
    "v_exp_f32 v205, v205\n" \
    "v_exp_f32 v206, v206\n" \
    "v_cvt_pk_bf16_f32 v230, v204, v205\n" \
    "v_exp_f32 v207, v207\n" \
    "v_mfma_f32_32x32x16_bf16 v[96:111], v[184:187], v[224:227], v[96:111]\n" \
    "s_waitcnt vmcnt(12)\n" \
    "v_cvt_pk_bf16_f32 v231, v206, v207\n" \
    "v_exp_f32 v208, v208\n" \
    "v_exp_f32 v209, v209\n" \
    "v_exp_f32 v210, v210\n" \
    "v_cvt_pk_bf16_f32 v232, v208, v209\n" \
    "v_exp_f32 v211, v211\n" \
    "v_mfma_f32_32x32x16_bf16 v[192:207], v[128:131], v[32:35], 0\n" \
    "v_cvt_pk_bf16_f32 v233, v210, v211\n" \
    "v_exp_f32 v212, v212\n" \
    "v_exp_f32 v213, v213\n" \
    "v_exp_f32 v214, v214\n" \
    "v_cvt_pk_bf16_f32 v234, v212, v213\n" \
    "v_exp_f32 v215, v215\n" \
    "v_mfma_f32_32x32x16_bf16 v[192:207], v[132:135], v[36:39], v[192:207]\n" \
    "s_sub_u32 %[cn], %[cn], 1\n" \
    "v_cvt_pk_bf16_f32 v235, v214, v215\n" \
    "v_exp_f32 v216, v216\n" \
    "v_exp_f32 v217, v217\n" \
    "v_exp_f32 v218, v218\n" \
    "v_cvt_pk_bf16_f32 v236, v216, v217\n" \
    "v_exp_f32 v219, v219\n" \
    "v_mfma_f32_32x32x16_bf16 v[96:111], v[188:191], v[228:231], v[96:111]\n" \
    "s_cmp_lg_u32 %[cn], 0\n" \
    "v_cvt_pk_bf16_f32 v237, v218, v219\n" \
    "v_exp_f32 v220, v220\n" \
    "v_exp_f32 v221, v221\n" \
    "v_exp_f32 v222, v222\n" \
    "v_cvt_pk_bf16_f32 v238, v220, v221\n" \
    "v_exp_f32 v223, v223\n" \
    "s_cbranch_scc1 ATT_LOOP_%=\n" \
    "v_mfma_f32_32x32x16_bf16 v[112:127], v[184:187], v[232:235], v[112:127]\n" \
    "v_cvt_pk_bf16_f32 v239, v222, v223\n" \
    "s_nop 1\n" \
    "v_mfma_f32_32x32x16_bf16 v[112:127], v[188:191], v[236:239], v[112:127]\n" \
    "s_waitcnt vmcnt(0)\n" \
    "s_nop 7\n" \
    "s_nop 3\n" \
    ""
#define ATTN_CLOB "v128", "v129", "v130", "v131", "v132", "v133", "v134", "v135", "v136", "v137", "v138", "v139", "v140", "v141", "v142", "v143", "v144", "v145", "v146", "v147", "v148", "v149", "v150", "v151", "v152", "v153", "v154", "v155", "v156", "v157", "v158", "v159", "v160", "v161", "v162", "v163", "v164", "v165", "v166", "v167", "v168", "v169", "v170", "v171", "v172", "v173", "v174", "v175", "v176", "v177", "v178", "v179", "v180", "v181", "v182", "v183", "v184", "v185", "v186", "v187", "v188", "v189", "v190", "v191", "v192", "v193", "v194", "v195", "v196", "v197", "v198", "v199", "v200", "v201", "v202", "v203", "v204", "v205", "v206", "v207", "v208", "v209", "v210", "v211", "v212", "v213", "v214", "v215", "v216", "v217", "v218", "v219", "v220", "v221", "v222", "v223", "v224", "v225", "v226", "v227", "v228", "v229", "v230", "v231", "v232", "v233", "v234", "v235", "v236", "v237", "v238", "v239", "v244", "v245", "v246", "v247"

DI rsrc_t mkrs(const void* p, unsigned bytes) { return __builtin_amdgcn_make_buffer_rsrc((void*)p, (short)0, (int)bytes, 0x00020000); }
DI void dma1(unsigned m0v, unsigned vo, rsrc_t rs, unsigned so) {
    asm volatile("s_mov_b32 m0, %0\n\ts_nop 0\n\tbuffer_load_dwordx4 %1, %2, %3 offen lds" ::"s"(m0v), "v"(vo), "s"(rs), "s"(so) : "memory");
}
DI void dma1c(unsigned m0v, unsigned vo, rsrc_t rs, unsigned so) {
    asm volatile("s_mov_b32 m0, %0\n\ts_nop 0\n\tbuffer_load_dwordx4 %1, %2, %3 offen sc1 lds" ::"s"(m0v), "v"(vo), "s"(rs), "s"(so) : "memory");
}
DI void dma2(unsigned m0v, unsigned v0, unsigned v1, rsrc_t rs, unsigned so) {
    asm volatile("s_mov_b32 m0, %0\n\ts_nop 0\n\tbuffer_load_dwordx4 %1, %3, %4 offen lds\n\tbuffer_load_dwordx4 %2, %3, %4 offen offset:1024 lds"
                 ::"s"(m0v), "v"(v0), "v"(v1), "s"(rs), "s"(so) : "memory");
}
DI void dma4(unsigned m0v, unsigned v0, unsigned v1, unsigned v2, unsigned v3, rsrc_t rs, unsigned so) {
    asm volatile("s_mov_b32 m0, %0\n\ts_nop 0\n\tbuffer_load_dwordx4 %1, %5, %6 offen lds\n\tbuffer_load_dwordx4 %2, %5, %6 offen offset:1024 lds\n\t"
                 "buffer_load_dwordx4 %3, %5, %6 offen offset:2048 lds\n\tbuffer_load_dwordx4 %4, %5, %6 offen offset:3072 lds"
                 ::"s"(m0v), "v"(v0), "v"(v1), "v"(v2), "v"(v3), "s"(rs), "s"(so) : "memory");
}
#define WAITV(n) asm volatile("s_waitcnt vmcnt(" #n ")" ::: "memory")
#define BAR() asm volatile("s_waitcnt lgkmcnt(0)\n\ts_barrier" ::: "memory")
#define RLX __ATOMIC_RELAXED, __HIP_MEMORY_SCOPE_AGENT
template <class T> DI T ldsr(unsigned a) { return *(const LAS T*)a; }
template <class T> DI void ldsw(unsigned a, T v) { *(LAS T*)a = v; }
DI s16x4 trr(unsigned a) { return __builtin_amdgcn_ds_read_tr16_b64_v4i16((LAS s16x4*)a); }
DI bf16x8 tr8(unsigned a0, unsigned a1) { s16x4 lo = trr(a0), hi = trr(a1); return __builtin_bit_cast(bf16x8, __builtin_shufflevector(lo, hi, 0, 1, 2, 3, 4, 5, 6, 7)); }
DI unsigned pk(float lo, float hi) { f32x2 v = {lo, hi}; return __builtin_bit_cast(unsigned, __builtin_convertvector(v, bf16x2)); }
DI float bflo(unsigned u) { return __uint_as_float(u << 16); }
DI float bfhi(unsigned u) { return __uint_as_float(u & 0xffff0000u); }
DI f32x16 mf32(bf16x8 a, bf16x8 b, f32x16 c) { return __builtin_amdgcn_mfma_f32_32x32x16_bf16(a, b, c, 0, 0, 0); }
DI f32x4 mf16(bf16x8 a, bf16x8 b, f32x4 c) { return __builtin_amdgcn_mfma_f32_16x16x32_bf16(a, b, c, 0, 0, 0); }
DI float uphalf(float v) {
    auto r = __builtin_amdgcn_permlane32_swap(__float_as_uint(v), __float_as_uint(v), false, false); return __uint_as_float(r[1]); }
DI float sigm(float v) { return __builtin_amdgcn_rcpf(1.f + __builtin_amdgcn_exp2f(-1.4426950408889634f * v)); }
template <int SH> DI u64 pollw(const char* p, u64 want) {
    u64 v;
    for (unsigned n = 0;; ++n) {
        v = __hip_atomic_load((const GAS u64*)p, RLX);
        if (__all((v >> SH) == want) || n > SPIN_CAP) break;
        __builtin_amdgcn_s_sleep(1);
    }
    asm volatile("" ::: "memory");
    return v;
}
DI void stflag(char* p, u64 v) { __hip_atomic_store((GAS u64*)p, v, RLX); }
DI unsigned top8(unsigned a, unsigned b, int l) {
    unsigned res = 0;
#pragma unroll 1
    for (int e = 0; e < 8; ++e) {
        unsigned m = a > b ? a : b;
        m = max(m, (unsigned)__builtin_amdgcn_update_dpp(0, (int)m, 0x111, 0xf, 0xf, true));
        m = max(m, (unsigned)__builtin_amdgcn_update_dpp(0, (int)m, 0x112, 0xf, 0xf, true));
        m = max(m, (unsigned)__builtin_amdgcn_update_dpp(0, (int)m, 0x114, 0xf, 0xf, true));
        m = max(m, (unsigned)__builtin_amdgcn_update_dpp(0, (int)m, 0x118, 0xf, 0xf, true));
        m = max(m, (unsigned)__builtin_amdgcn_update_dpp(0, (int)m, 0x142, 0xa, 0xf, true));
        m = max(m, (unsigned)__builtin_amdgcn_update_dpp(0, (int)m, 0x143, 0xc, 0xf, true));
        const unsigned w = (unsigned)__builtin_amdgcn_readlane((int)m, 63);
        a = a == w ? 0u : a; b = b == w ? 0u : b;
        res = l == e ? w : res;
    }
    return res;
}
DI unsigned hsync(unsigned addr, bool wait, int l) {
    unsigned old = 0;
    asm volatile("s_waitcnt lgkmcnt(0)" ::: "memory");
    if (l == 0) old = __hip_atomic_fetch_add((LAS unsigned*)addr, 1u, __ATOMIC_RELAXED, __HIP_MEMORY_SCOPE_WORKGROUP);
    old = __builtin_amdgcn_readfirstlane(old);
    if (wait)
        for (unsigned n = 0; n < (SPIN_CAP << 3); ++n) {
            if (__builtin_amdgcn_readfirstlane(__hip_atomic_load((LAS unsigned*)addr, __ATOMIC_RELAXED, __HIP_MEMORY_SCOPE_WORKGROUP)) >= 4u) break;
            __builtin_amdgcn_s_sleep(2);
        }
    asm volatile("" ::: "memory");
    return old;
}
#define STAMP(k)
#define STAMPC(k)
#define DUMP()

DI void epilogue(char* out, int wv, int l, int t, int idx, u32x4 xres) {
    const int tok = 4 * wv + (l >> 4), ch = l & 15, ct = idx & 3;
    const u32x4 a = ldsr<u32x4>(L_T + tok * 272 + ch * 16);
    const float g = ldsr<float>(L_GS + tok * 4), g1 = 1.f - g;
    const int e = ldsr<int>(L_MAP + tok * 4);
    f32x4 y0 = {0.f, 0.f, 0.f, 0.f}, y1 = y0;
    if (e >= 0) { y0 = ldsr<f32x4>(L_YS + e * 512 + ch * 32); y1 = ldsr<f32x4>(L_YS + e * 512 + ch * 32 + 16); }
    u32x4 o;
    o[0] = pk(g * bflo(a[0]) + g1 * (bflo(xres[0]) + y0[0]), g * bfhi(a[0]) + g1 * (bfhi(xres[0]) + y0[1]));
    o[1] = pk(g * bflo(a[1]) + g1 * (bflo(xres[1]) + y0[2]), g * bfhi(a[1]) + g1 * (bfhi(xres[1]) + y0[3]));
    o[2] = pk(g * bflo(a[2]) + g1 * (bflo(xres[2]) + y1[0]), g * bfhi(a[2]) + g1 * (bfhi(xres[2]) + y1[1]));
    o[3] = pk(g * bflo(a[3]) + g1 * (bflo(xres[3]) + y1[2]), g * bfhi(a[3]) + g1 * (bfhi(xres[3]) + y1[3]));
    if (ct < 3 || ch < 8) *(u32x4*)(out + (size_t)(t * 128 + (idx >> 2) * 32 + tok) * 896 + ct * 256 + ch * 16) = o;
}
DI u32x4 load_res(const char* x, int wv, int l, int t, int idx) {
    const int tok = 4 * wv + (l >> 4), ch = l & 15, ct = idx & 3;
    const int c = (ct < 3 || ch < 8) ? ch : 0;
    return *(const u32x4*)(x + (size_t)(t * 128 + (idx >> 2) * 32 + tok) * 896 + ct * 256 + c * 16);
}

__global__ __launch_bounds__(512, 2) void fused_kernel(const char* __restrict__ x, const char* __restrict__ wqkv, const char* __restrict__ wo,
                                                       const char* __restrict__ wr, const char* __restrict__ wgate, const char* __restrict__ wup,
                                                       const char* __restrict__ wdown, const char* __restrict__ gw, char* __restrict__ out, char* ws) {
    const int b = blockIdx.x, xcd = b & 7, i = b >> 3;
    const int t = (xcd & 3) * 4 + (i & 3), idx = (i >> 2) * 2 + (xcd >> 2);
    const int wv = __builtin_amdgcn_readfirstlane(threadIdx.x >> 6), l = threadIdx.x & 63;
    const int h2 = l >> 5, r = l & 31, r8 = l >> 3, c8 = l & 7;
    const int tq = (l & 15) >> 2, tp = l & 3, blk = (l >> 4) & 1;
    const bool spare = idx >= 14;
    STAMP(0);
    if (wv < 4) {
        const rsrc_t rx = mkrs(x, 2048 * 896);
        const unsigned xrow = (t * 128 + wv * 32 + r8) * 896;
        const unsigned xa = xrow + ((c8 ^ (r8 >> 1)) << 4), xb = xrow + ((c8 ^ (r8 >> 1) ^ 4) << 4);
        const unsigned xv0 = xa, xv1 = xb + 6144, xv2 = xa + 2 * 6144, xv3 = xb + 3 * 6144;
        const int rot = (i >> 2) == 7 ? 0 : (i >> 2);
        const unsigned xr = L_XR + wv * 16384;
#define CH(p) ((p) + rot >= 7 ? (p) + rot - 7 : (p) + rot)
#pragma unroll
        for (int p = 0; p < 4; ++p) dma4(xr + p * 4096, xv0, xv1, xv2, xv3, rx, CH(p) * 128);
        STAMP(1);
        const rsrc_t rkv = mkrs(ws + WS_KV, 4u << 20);
        const bool isK = idx < 7;
        unsigned so[8];
#pragma unroll
        for (int q = 0; q < 8; ++q) { const unsigned G = 16 * idx + 2 * q + h2, hh = (G * 147) >> 10; so[q] = hh * 262144 + (G - 7 * hh) * 256 + r * 8; }
        if (!isK) {
#pragma unroll
            for (int n = 0; n < 2; ++n) { const unsigned C = 64 * (idx - 7) + 32 * n + r, hh = (C * 2341) >> 16; so[n] = hh * 262144 + 2048 + h2 * 512 + (C - 28 * hh) * 16; }
        }
        unsigned xad[4];
#pragma unroll
        for (int s = 0; s < 4; ++s) xad[s] = xr + r * 128 + (((((r >> 1) & 7) ^ h2) << 4) ^ (32 * s));
        const unsigned wad = L_WKV + (8 * h2 + tq) * 128 + (((blk * 2 + (tp >> 1)) ^ ((tq >> 1) << 2)) << 4) + (tp & 1) * 8;
        bf16x8 xf[28];
        f32x16 acc0 = {}, acc1 = {};
        WAITV(12);
        BAR();
        STAMP(2);
        const u64 tag = ldsr<u64>(L_CTL);
#define RING(KIND) _Pragma("unroll") for (int p = 0; p < 7; ++p) { \
            if (p == 1 || p == 2 || p == 3) WAITV(12); else if (p == 4) WAITV(8); else if (p == 5) WAITV(4); else if (p == 6) WAITV(0); \
            _Pragma("unroll") for (int s = 0; s < 4; ++s) xf[4 * p + s] = ldsr<bf16x8>(xad[s] + (p & 3) * 4096); \
            const unsigned w0 = wad + CH(p) * 8192, w1 = w0 ^ 64; \
            _Pragma("unroll") for (int s = 0; s < 4; ++s) { \
                if (KIND == 0) { acc0 = mf32(tr8(w0 + s * 2048, w0 + s * 2048 + 512), xf[4 * p + s], acc0); acc1 = mf32(tr8(w1 + s * 2048, w1 + s * 2048 + 512), xf[4 * p + s], acc1); } \
                if (KIND == 1) { acc0 = mf32(xf[4 * p + s], tr8(w0 + s * 2048, w0 + s * 2048 + 512), acc0); acc1 = mf32(xf[4 * p + s], tr8(w1 + s * 2048, w1 + s * 2048 + 512), acc1); } \
            } \
            if (p < 3) { asm volatile("s_waitcnt lgkmcnt(0)" ::: "memory"); dma4(xr + p * 4096, xv0, xv1, xv2, xv3, rx, CH(p + 4) * 128); } \
        }
        if (isK) { RING(0) } else if (!spare) { RING(1) } else { RING(2) }
        STAMP(3);
        const unsigned tso = (t * 4 + wv) * 4096;
        if (isK) {
#pragma unroll
            for (int q = 0; q < 4; ++q) {
                __builtin_amdgcn_raw_buffer_store_b64((u32x2){pk(acc0[4 * q], acc0[4 * q + 1]), pk(acc0[4 * q + 2], acc0[4 * q + 3])}, rkv, so[q], tso, 16);
                __builtin_amdgcn_raw_buffer_store_b64((u32x2){pk(acc1[4 * q], acc1[4 * q + 1]), pk(acc1[4 * q + 2], acc1[4 * q + 3])}, rkv, so[4 + q], tso, 16);
            }
        } else if (!spare) {
#pragma unroll
            for (int s = 0; s < 2; ++s) {
                __builtin_amdgcn_raw_buffer_store_b128((u32x4){pk(acc0[8 * s], acc0[8 * s + 1]), pk(acc0[8 * s + 2], acc0[8 * s + 3]), pk(acc0[8 * s + 4], acc0[8 * s + 5]), pk(acc0[8 * s + 6], acc0[8 * s + 7])}, rkv, so[0] + s * 1024, tso, 16);
                __builtin_amdgcn_raw_buffer_store_b128((u32x4){pk(acc1[8 * s], acc1[8 * s + 1]), pk(acc1[8 * s + 2], acc1[8 * s + 3]), pk(acc1[8 * s + 4], acc1[8 * s + 5]), pk(acc1[8 * s + 6], acc1[8 * s + 7])}, rkv, so[1] + s * 1024, tso, 16);
            }
        }
        BAR();
        WAITV(0);
        BAR();
        if (!spare && wv == 0 && l == 0) stflag(ws + WS_KVFLAG + (t * 16 + idx) * 128, tag);
        STAMP(4);
        BAR();
        const unsigned qad = L_WQ + (8 * h2 + tq) * 64 + ((((idx * 56) & 15) + blk * 32 + tp * 8) & 63);
        f32x16 aq = {};
#pragma unroll
        for (int p = 0; p < 7; ++p) {
            const unsigned q0 = qad + CH(p) * 4096;
#pragma unroll
            for (int s = 0; s < 4; ++s) aq = mf32(tr8(q0 + s * 1024, q0 + s * 1024 + 256), xf[4 * p + s], aq);
        }
        const float gate = sigm(aq[13]);
        if (idx == 14 && h2) {
            const unsigned u = pk(aq[12], 0.f) & 0xffffu;
            const unsigned o = (u & 0x7fffu) > 0x7f80u ? 0u : (u ^ ((u & 0x8000u) ? 0xffffu : 0x8000u));
            ldsw<unsigned>(L_KEY + (wv * 32 + r) * 4, (o << 11) | (2047 - (t * 128 + wv * 32 + r)));
        }
        constexpr float QS = 0.18898223650461361f * 1.4426950408889634f;
        {
            u32x4 q0 = {pk(aq[0] * QS, aq[1] * QS), pk(aq[2] * QS, aq[3] * QS), pk(aq[4] * QS, aq[5] * QS), pk(aq[6] * QS, aq[7] * QS)};
            u32x4 q1 = {pk(aq[8] * QS, aq[9] * QS), pk(aq[10] * QS, aq[11] * QS), pk(aq[12] * QS, aq[13] * QS), pk(aq[14] * QS, aq[15] * QS)};
            ldsw<u32x4>(L_QX + wv * 2048 + l * 16, q0);
            ldsw<u32x4>(L_QX + wv * 2048 + 1024 + l * 16, q1);
        }
        BAR();
        STAMP(5);
        u32x4 q00, q01, q10, q11, q20, q21, q30, q31;
        q00 = ldsr<u32x4>(L_QX + wv * 2048 + l * 16);             q01 = ldsr<u32x4>(L_QX + wv * 2048 + 1024 + l * 16);
        q10 = ldsr<u32x4>(L_QX + ((wv + 1) & 3) * 2048 + l * 16); q11 = ldsr<u32x4>(L_QX + ((wv + 1) & 3) * 2048 + 1024 + l * 16);
        q20 = ldsr<u32x4>(L_QX + ((wv + 2) & 3) * 2048 + l * 16); q21 = ldsr<u32x4>(L_QX + ((wv + 2) & 3) * 2048 + 1024 + l * 16);
        q30 = ldsr<u32x4>(L_QX + ((wv + 3) & 3) * 2048 + l * 16); q31 = ldsr<u32x4>(L_QX + ((wv + 3) & 3) * 2048 + 1024 + l * 16);
        const u32x4 xres = load_res(x, wv, l, t, idx);
        {
            const int js = l & 3, j = (((7 * idx + ((js & 1) ? 6 : 0)) >> 4) + ((js & 2) ? 7 : 0));
            pollw<0>(ws + WS_KVFLAG + ((4 * wv + ((l >> 2) & 3)) * 16 + j) * 128, tag);
        }
        STAMP(6); STAMPC(13);
        __builtin_amdgcn_s_setprio(2);
        const rsrc_t ratt = mkrs(ws + WS_KV + idx * 262144 + wv * 65536, 65536);
        f32x16 o0, o1, o2, o3; float m0, m1, m2, m3;
#pragma unroll 1
        for (unsigned mode = 0;; ++mode) {
            asm volatile("" : "+s"(mode));
            unsigned s_so, s_sk, s_s2, s_cn;
            asm volatile(ATTN_ASM
                : "=&{v[64:79]}"(o0), "=&{v[80:95]}"(o1), "=&{v[96:111]}"(o2), "=&{v[112:127]}"(o3),
                  "=&{v240}"(m0), "=&{v241}"(m1), "=&{v242}"(m2), "=&{v243}"(m3),
                  "+{v[36:39]}"(q01), "+{v[44:47]}"(q11), "+{v[52:55]}"(q21), "+{v[60:63]}"(q31),
                  [so] "=&s"(s_so), [sk] "=&s"(s_sk), [s2] "=&s"(s_s2), [cn] "=&s"(s_cn)
                : "{v[32:35]}"(q00), "{v[40:43]}"(q10), "{v[48:51]}"(q20), "{v[56:59]}"(q30),
                  [vk] "v"(l * 8), [vv] "v"(l * 16), [rs] "s"(ratt), [st] "s"((t & 3) * 16384), [md] "s"(mode)
                : ATTN_CLOB, "scc", "memory");
            const float lo = 8.6736174e-19f, hi = 1.1529215e18f;
            const bool ok = o0[12] > lo && o0[12] < hi && o1[12] > lo && o1[12] < hi && o2[12] > lo && o2[12] < hi && o3[12] > lo && o3[12] < hi;
            if (mode || (__ballot(!ok) >> 32) == 0) break;
        }
        STAMP(7); STAMPC(14);
        int l_ = l; asm volatile("" : "+v"(l_));

        {
        const int l = l_, h2 = l >> 5, r = l & 31, tq = (l & 15) >> 2, tp = l & 3, blk = (l >> 4) & 1;
        BAR();
        if (h2) { o1[13] = m1; o2[13] = m2; o3[13] = m3; }
#pragma unroll
        for (int q = 0; q < 4; ++q) {
            ldsw<f32x4>(L_MS + (((wv + 1) & 3) * 3 + 0) * 4096 + q * 1024 + l * 16, (f32x4){o1[4 * q], o1[4 * q + 1], o1[4 * q + 2], o1[4 * q + 3]});
            ldsw<f32x4>(L_MS + (((wv + 2) & 3) * 3 + 1) * 4096 + q * 1024 + l * 16, (f32x4){o2[4 * q], o2[4 * q + 1], o2[4 * q + 2], o2[4 * q + 3]});
            ldsw<f32x4>(L_MS + (((wv + 3) & 3) * 3 + 2) * 4096 + q * 1024 + l * 16, (f32x4){o3[4 * q], o3[4 * q + 1], o3[4 * q + 2], o3[4 * q + 3]});
        }
        BAR();
        {
            f32x4 pa[4], pb[4], pc[4];
#pragma unroll
            for (int q = 0; q < 4; ++q) {
                pa[q] = ldsr<f32x4>(L_MS + (wv * 3 + 0) * 4096 + q * 1024 + l * 16);
                pb[q] = ldsr<f32x4>(L_MS + (wv * 3 + 1) * 4096 + q * 1024 + l * 16);
                pc[q] = ldsr<f32x4>(L_MS + (wv * 3 + 2) * 4096 + q * 1024 + l * 16);
            }
            const float ma = uphalf(pa[3][1]), mb = uphalf(pb[3][1]), mc = uphalf(pc[3][1]);
            const float mm = fmaxf(fmaxf(m0, ma), fmaxf(mb, mc));
            float f0 = __builtin_amdgcn_exp2f(m0 - mm), fa = __builtin_amdgcn_exp2f(ma - mm), fb = __builtin_amdgcn_exp2f(mb - mm), fc = __builtin_amdgcn_exp2f(mc - mm);
            const float inv = __builtin_amdgcn_rcpf(uphalf(f0 * o0[12] + fa * pa[3][0] + fb * pb[3][0] + fc * pc[3][0]));
            f0 *= inv; fa *= inv; fb *= inv; fc *= inv;
            float v[16];
#pragma unroll
            for (int k = 0; k < 16; ++k) v[k] = f0 * o0[k] + fa * pa[k >> 2][k & 3] + fb * pb[k >> 2][k & 3] + fc * pc[k >> 2][k & 3];
            const rsrc_t ro = mkrs(ws + WS_O, 2u << 20);
            const unsigned oso = ((t * 4 + wv) * 16 + idx) * 2048;
            __builtin_amdgcn_raw_buffer_store_b128((u32x4){pk(v[0], v[1]), pk(v[2], v[3]), pk(v[4], v[5]), pk(v[6], v[7])}, ro, l * 16, oso, 16);
            __builtin_amdgcn_raw_buffer_store_b128((u32x4){pk(v[8], v[9]), pk(v[10], v[11]), h2 ? 0u : pk(v[12], v[13]), h2 ? 0u : pk(v[14], v[15])}, ro, l * 16 + 1024, oso, 16);
        }
        STAMP(8);
        const bool act = (idx & 3) < 3 || wv < 2;
        bf16x8 wf[32];
        {
            const unsigned a0 = L_WO + (4 * h2 + tq) * 256 + (((wv * 4 + blk * 2 + (tp >> 1)) ^ (tq << 2)) << 4) + (tp & 1) * 8;
            const unsigned a1 = a0 + 8 * 28 * 256, a2 = a1 - h2 * 1024;
#pragma unroll
            for (int k = 0; k < 32; ++k) {
                const int hh = k >> 1, s = k & 1;
                const unsigned base = hh < 8 ? a0 : a1, off = (28 * (hh & 7) + 16 * s) * 256;
                wf[k] = tr8(base + off, (k == 31 ? a2 : base) + off + 2048);
            }
        }
        WAITV(0);
        BAR();
        if (wv == 0 && l == 0) stflag(ws + WS_OFLAG + (t * 16 + idx) * 128, tag);
        STAMP(9);
        if (wv == (idx >> 2) && h2) ldsw<float>(L_GS + r * 4, gate);
        BAR();
        STAMP(10);
        f32x16 ac = {};
        if (act) {
#pragma unroll
            for (int k = 0; k < 32; ++k) ac = mf32(wf[k], ldsr<bf16x8>(L_OF + k * 1024 + l * 16), ac);
#pragma unroll
            for (int q = 0; q < 4; ++q) ldsw<u32x2>(L_T + r * 272 + (32 * wv + 8 * q + 4 * h2) * 2, (u32x2){pk(ac[4 * q], ac[4 * q + 1]), pk(ac[4 * q + 2], ac[4 * q + 3])});
        }
        BAR();
        STAMP(11);
        epilogue(out, wv, l, t, idx, xres);
        STAMP(12); DUMP();
        }
    } else {
        const int hw = wv - 4;
        __builtin_amdgcn_s_setprio(3);
        const rsrc_t rw = mkrs(wqkv, 448 * 2688);
        const unsigned wqv = (l >> 2) * 2688 + ((idx * 56) & ~15) + (l & 3) * 16;
        if (!spare) {
            const unsigned v0 = r8 * 2688 + 896 + idx * 128 + ((c8 ^ (((r8 >> 1) & 1) << 2)) << 4), v1 = v0 + 21504 - 1024;
            int g = (t & 3) * 2;
#pragma unroll 1
            for (int n = 0; n < 7; ++n) { const int p = 14 * hw + 2 * g; dma2(L_WKV + p * 1024, v0, v1, rw, p * 21504); g = g == 6 ? 0 : g + 1; }
        } else {
#pragma unroll 1
            for (int n = 0; n < 7; ++n) { const int p = 7 * hw + n; dma1(L_WQ + p * 1024, wqv, rw, p * 43008); }
        }
        __builtin_amdgcn_s_setprio(0);
        STAMP(1);
        const u64 tag = __hip_atomic_load((const GAS u64*)(ws + WS_EPOCH), RLX) + 1;
        const int lc = l < 56 ? l : 55;
        const unsigned wr2 = *(const unsigned*)(wr + (112 * hw + 2 * lc) * 2), gw2 = *(const unsigned*)(gw + (112 * hw + 2 * lc) * 2);
        if (!spare) {
            const int jj = idx < 7 ? idx : idx - 7, hh = (16 * jj) / 7 + (l >> 4), li = l & 15;
            const rsrc_t rkv = mkrs(ws + WS_KV, 4u << 20);
            const unsigned one = (idx < 7 || (li & 3) == 0) ? 0x3f803f80u : 0u, w1 = idx < 7 ? 0x00003f80u : one;
            const unsigned off = idx < 7 ? 7 * 256 + li * 16 : 2048 + (li >> 3) * 1024 + ((li >> 2) & 1) * 512 + (28 + (li & 3)) * 16;
            if (hh < (16 * (jj + 1)) / 7) __builtin_amdgcn_raw_buffer_store_b128((u32x4){one, w1, one, w1}, rkv, hh * 262144 + off, (t * 4 + hw) * 4096, 16);
        }
        if (hw == 0 && l == 0) { ldsw<u64>(L_CTL, tag); ldsw<u32x4>(L_CTL + 16, (u32x4){0u, 0u, 0u, 0u}); }
        const unsigned pad = L_WQ + (112 * hw + 2 * lc) * 64 + ((((idx * 56) & 15) + 56) & 63);
        const u32x2 pad0 = {(wr2 & 0xffffu) | (gw2 << 16), 0u}, pad1 = {(wr2 >> 16) | (gw2 & 0xffff0000u), 0u};
        WAITV(0);
        if (spare) { ldsw<u32x2>(pad, pad0); ldsw<u32x2>(pad + 64, pad1); }
        BAR();
        STAMP(2);
        const int mid = idx == 15 ? t : 16 + t;
        const bool moe = spare && mid < 28;
        const rsrc_t rg = mkrs((hw >> 1) ? wup : wgate, 448 * 3584);
        const unsigned gph = (r8 >> 1) & 1;
        const unsigned gv0 = (224 * (hw & 1) + r8) * 3584 + mid * 128 + ((c8 ^ (gph << 1)) << 4);
        const unsigned gv1 = (224 * (hw & 1) + r8 + 8) * 3584 + mid * 128 + ((c8 ^ ((gph | 2) << 1)) << 4) - 1024;
        const unsigned areaA = hw * 14336, areaB = L_XR + hw * 16384;
        if (moe) {
#pragma unroll 1
            for (int n = 0; n < 6; ++n) dma2(areaA + n * 2048, gv0, gv1, rg, n * 16 * 3584);
        }
        BAR();
        if (!spare) {
#pragma unroll 1
            for (int n = 0; n < 7; ++n) { const int p = 7 * hw + n; dma1(L_WQ + p * 1024, wqv, rw, p * 43008); }
        } else if (moe) {
#pragma unroll 1
            for (int n = 0; n < 8; ++n) dma2(areaB + n * 2048, gv0, gv1, rg, (n + 6) * 16 * 3584);
        }
        BAR();
        if (!spare) {
            WAITV(0);
            ldsw<u32x2>(pad, pad0); ldsw<u32x2>(pad + 64, pad1);
        }
        STAMP(4);
        BAR();
        BAR();
        STAMP(5);
        if (idx == 14 && hw == 0) {
            const unsigned k = top8(ldsr<unsigned>(L_KEY + l * 4), ldsr<unsigned>(L_KEY + 256 + l * 4), l);
            if (l < 8) stflag(ws + WS_CAND + t * 128 + l * 8, (tag << 27) | k);
            if (t == 15) {
                const char* cp = ws + WS_CAND + (l >> 3) * 128 + (l & 7) * 8;
                const unsigned ka = (unsigned)pollw<27>(cp, tag & TAG37) & 0x7ffffffu, kb = (unsigned)pollw<27>(cp + 1024, tag & TAG37) & 0x7ffffffu;
                const unsigned s = top8(ka, kb, l);
                if (l < 8) stflag(ws + WS_SEL + l * 8, (tag << 27) | s);
            }
        }
        const u32x4 xres = load_res(x, wv, l, t, idx);
        const rsrc_t rwo = mkrs(wo, 448 * 896);
        const unsigned wov0 = (l >> 4) * 896 + (idx & 3) * 256 + (((l & 15) ^ ((l >> 4) << 2)) << 4), wov1 = wov0 + 3584 - 1024;
        unsigned key;
        if (moe) {
            bf16x8 gf[28], df[14];
            const int g4 = l >> 4;
            const unsigned gad = (8 * g4 + tq) * 128 + (tp >> 1) * 16 + (tp & 1) * 8, gx = ((tq >> 1) | ((g4 & 1) << 1)) << 5;
            WAITV(16);
#pragma unroll
            for (int ks = 0; ks < 3; ++ks)
#pragma unroll
                for (int c = 0; c < 4; ++c) { const unsigned a = areaA + gad + ((c << 5) ^ gx) + ks * 4096; gf[ks * 4 + c] = tr8(a, a + 512); }
            WAITV(0);
#pragma unroll
            for (int ks = 3; ks < 7; ++ks)
#pragma unroll
                for (int c = 0; c < 4; ++c) { const unsigned a = areaB + gad + ((c << 5) ^ gx) + (ks - 3) * 4096; gf[ks * 4 + c] = tr8(a, a + 512); }
            asm volatile("s_waitcnt lgkmcnt(0)" ::: "memory");
            const rsrc_t rd = mkrs(wdown, 1792 * 896);
            const unsigned dbase = (64 * mid + g4) * 896 + hw * 224;
            const unsigned dv0 = dbase + (((l & 15) ^ (g4 << 1)) << 4), dv1 = dv0 + 3584 - 1024;
            const unsigned dv2 = dbase + (((l & 15) ^ ((g4 | 4) << 1)) << 4) + 2 * (3584 - 1024), dv3 = dv2 + 3584 - 1024;
#pragma unroll 1
            for (int n = 0; n < 4; ++n) dma4(areaB + n * 4096, dv0, dv1, dv2, dv3, rd, n * 16 * 896);
            WAITV(0);
            const unsigned dad = areaB + (8 * g4 + tq) * 256 + (tp >> 1) * 16 + (tp & 1) * 8, dx = (tq | ((g4 & 1) << 2)) << 5;
#pragma unroll
            for (int ks = 0; ks < 2; ++ks)
#pragma unroll
                for (int c = 0; c < 7; ++c) { const unsigned a = dad + ((c << 5) ^ dx) + ks * 8192; df[ks * 7 + c] = tr8(a, a + 1024); }
            STAMP(6);
            key = (unsigned)pollw<27>(ws + WS_SEL + (l & 7) * 8, tag & TAG37) & 0x7ffffffu;
            const char* xs = x + (size_t)(2047 - (key & 2047u)) * 896 + (224 * (hw & 1) + 8 * g4) * 2;
            f32x4 pa[4] = {};
#pragma unroll
            for (int ks = 0; ks < 7; ++ks) {
                const bf16x8 xb = *(const bf16x8*)(xs + ks * 64);
#pragma unroll
                for (int c = 0; c < 4; ++c) pa[c] = mf16(gf[ks * 4 + c], xb, pa[c]);
            }
#pragma unroll
            for (int c = 0; c < 4; ++c) ldsw<f32x4>(L_EX1 + hw * 4096 + c * 1024 + l * 16, pa[c]);
            hsync(L_CTL + 16, true, l);
            const f32x4 ga = ldsr<f32x4>(L_EX1 + hw * 1024 + l * 16) + ldsr<f32x4>(L_EX1 + 4096 + hw * 1024 + l * 16);
            const f32x4 ua = ldsr<f32x4>(L_EX1 + 8192 + hw * 1024 + l * 16) + ldsr<f32x4>(L_EX1 + 12288 + hw * 1024 + l * 16);
            ldsw<u32x2>(L_EX2 + (l & 15) * 144 + (16 * hw + 4 * g4) * 2,
                        (u32x2){pk(ga[0] * sigm(ga[0]) * ua[0], ga[1] * sigm(ga[1]) * ua[1]), pk(ga[2] * sigm(ga[2]) * ua[2], ga[3] * sigm(ga[3]) * ua[3])});
            hsync(L_CTL + 20, true, l);
            const bf16x8 h0 = ldsr<bf16x8>(L_EX2 + (l & 15) * 144 + 16 * g4), h1 = ldsr<bf16x8>(L_EX2 + (l & 15) * 144 + 64 + 16 * g4);
            const rsrc_t ry = mkrs(ws + WS_YP, 28 * 14336);
#pragma unroll
            for (int c = 0; c < 7; ++c) {
                const f32x4 y = mf16(df[7 + c], h1, mf16(df[c], h0, (f32x4){0.f, 0.f, 0.f, 0.f}));
                if ((l & 15) < 8) __builtin_amdgcn_raw_buffer_store_b128(__builtin_bit_cast(u32x4, y), ry, (l & 15) * 1792 + (112 * hw + 16 * c + 4 * g4) * 4, mid * 14336, 16);
            }
            WAITV(0);
            if (hsync(L_CTL + 24, false, l) == 3 && l == 0) stflag(ws + WS_MFLAG + mid * 128, tag);
            STAMP(7);
        } else {
            __builtin_amdgcn_s_sleep(40);
        }
#pragma unroll 1
        for (int n = 0; n < 14; ++n) { const int p = 28 * hw + 2 * n; dma2(L_WO + p * 1024, wov0, wov1, rwo, p * 3584); WAITV(4); }
        if (!moe) key = (unsigned)pollw<27>(ws + WS_SEL + (l & 7) * 8, tag & TAG37) & 0x7ffffffu;
        const int tl = (2047 - (int)(key & 2047u)) - (t * 128 + (idx >> 2) * 32);
        const unsigned hit = (unsigned)__ballot(tl >= 0 && tl < 32) & 0xffu;
        f32x4 ya[2], yb[2];
        if (((hit >> hw) & 0x11u) || (b == 0 && hw == 0)) pollw<0>(ws + WS_MFLAG + (l < 28 ? l : 0) * 128, tag);
#pragma unroll
        for (int u = 0; u < 2; ++u) {
            const int e = hw + 4 * u;
            ya[u] = (f32x4){0.f, 0.f, 0.f, 0.f}; yb[u] = ya[u];
            if ((hit >> e) & 1) {
                const rsrc_t ry = mkrs(ws + WS_YP, 28 * 14336);
                const int cc = ((idx & 3) < 3 || (l & 15) < 8) ? (l & 15) : 0;
                const unsigned yo = (l >> 4) * 14336 + e * 1792 + ((idx & 3) * 128 + cc * 8) * 4;
#pragma unroll
                for (int p = 0; p < 7; ++p) {
                    ya[u] += __builtin_bit_cast(f32x4, __builtin_amdgcn_raw_buffer_load_b128(ry, yo, p * 4 * 14336, 16));
                    yb[u] += __builtin_bit_cast(f32x4, __builtin_amdgcn_raw_buffer_load_b128(ry, yo + 16, p * 4 * 14336, 16));
                }
                const unsigned ko = (unsigned)__builtin_amdgcn_readlane((int)key, e) >> 11;
                const float rs = sigm(bflo(ko ^ ((ko & 0x8000u) ? 0x8000u : 0xffffu)));
#pragma unroll
                for (int k = 0; k < 4; ++k) {
                    float a = ya[u][k], c = yb[u][k];
                    a += __shfl_xor(a, 16); a += __shfl_xor(a, 32); c += __shfl_xor(c, 16); c += __shfl_xor(c, 32);
                    ya[u][k] = a * rs; yb[u][k] = c * rs;
                }
            }
        }
        WAITV(0);
        STAMP(8);
        BAR();
        BAR();
        BAR();
        STAMP(9);
        if (hw == 1 && l < 32) {
            int e = -1;
#pragma unroll
            for (int k = 0; k < 8; ++k) e = __builtin_amdgcn_readlane(tl, k) == l ? k : e;
            ldsw<int>(L_MAP + l * 4, e);
        }
#pragma unroll
        for (int u = 0; u < 2; ++u)
            if (((hit >> (hw + 4 * u)) & 1) && l < 16) { ldsw<f32x4>(L_YS + (hw + 4 * u) * 512 + l * 32, ya[u]); ldsw<f32x4>(L_YS + (hw + 4 * u) * 512 + l * 32 + 16, yb[u]); }
        pollw<0>(ws + WS_OFLAG + (t * 16 + 4 * hw + (l & 3)) * 128, tag);
        STAMP(10);
        {
            const rsrc_t ro = mkrs(ws + WS_O, 2u << 20);
            const unsigned oso = ((t * 4 + (idx >> 2)) * 16 + 4 * hw) * 2048;
#pragma unroll
            for (int n = 0; n < 8; ++n) dma1c(L_OF + (8 * hw + n) * 1024, l * 16, ro, oso + n * 1024);
        }
        WAITV(0);
        BAR();
        if (b == 0 && hw == 0 && l == 0) stflag(ws + WS_EPOCH, tag);
        BAR();
        STAMP(11);
        epilogue(out, wv, l, t, idx, xres);
        STAMP(12); DUMP();
    }
}

extern "C" void kernel_launch(void* const* d_in, const int* in_sizes, int n_in, void* d_out, int out_size, void* d_ws, size_t ws_size,
                              hipStream_t stream) {
    (void)in_sizes; (void)n_in; (void)out_size; (void)ws_size;
    (void)hipFuncSetAttribute((const void*)fused_kernel, hipFuncAttributeMaxDynamicSharedMemorySize, LDS_BYTES);
    fused_kernel<<<256, 512, LDS_BYTES, stream>>>((const char*)d_in[0], (const char*)d_in[1], (const char*)d_in[2], (const char*)d_in[4],
                                                  (const char*)d_in[5], (const char*)d_in[6], (const char*)d_in[7], (const char*)d_in[8],
                                                  (char*)d_out, (char*)d_ws);
}
```

```cpp
#include <hip/hip_runtime.h>
#include <hip/hip_bf16.h>

#define LAS __attribute__((address_space(3)))
#define GAS __attribute__((address_space(1)))
#define DI __device__ __forceinline__
typedef float f32x16 __attribute__((ext_vector_type(16)));
typedef float f32x4 __attribute__((ext_vector_type(4)));
typedef float f32x2 __attribute__((ext_vector_type(2)));
typedef __bf16 bf16x8 __attribute__((ext_vector_type(8)));
typedef __bf16 bf16x2 __attribute__((ext_vector_type(2)));
typedef short s16x4 __attribute__((ext_vector_type(4)));
typedef unsigned u32x4 __attribute__((ext_vector_type(4)));
typedef unsigned u32x2 __attribute__((ext_vector_type(2)));
typedef unsigned long long u64;
typedef __amdgpu_buffer_rsrc_t rsrc_t;

constexpr unsigned WS_EPOCH = 0, WS_KVFLAG = 4096, WS_OFLAG = 36864, WS_CAND = 69632, WS_SEL = 71680, WS_MFLAG = 73728,
                   WS_YP = 81920, WS_KV = 1u << 20, WS_O = 5u << 20, WS_DBG = 8u << 20;
constexpr unsigned L_WKV = 0, L_XR = 57344, L_WQ = 122880, L_KEY = 151552, L_QX = 155136, L_CTL = 163328;
constexpr unsigned L_WO = 0, L_MS = 114688, L_EX1 = 122880, L_EX2 = 139264;
constexpr unsigned L_OF = 114688, L_T = 147456, L_GS = 156160, L_MAP = 156288, L_YS = 156416;
constexpr unsigned LDS_BYTES = 163840;
constexpr unsigned SPIN_CAP = 1u << 17;
constexpr u64 TAG37 = (1ull << 37) - 1;

#define ATTN_ASM \
    "s_mov_b32 %[sk], 0\n" \
    "s_mov_b32 %[so], %[st]\n" \
    "buffer_load_dwordx2 v[128:129], %[vk], %[rs], %[so] offen offset:0 sc1\n" \
    "buffer_load_dwordx2 v[130:131], %[vk], %[rs], %[so] offen offset:512 sc1\n" \
    "buffer_load_dwordx2 v[132:133], %[vk], %[rs], %[so] offen offset:1024 sc1\n" \
    "buffer_load_dwordx2 v[134:135], %[vk], %[rs], %[so] offen offset:1536 sc1\n" \
    "buffer_load_dwordx4 v[136:139], %[vv], %[rs], %[so] offen offset:2048 sc1\n" \
    "buffer_load_dwordx4 v[140:143], %[vv], %[rs], %[so] offen offset:3072 sc1\n" \
    "s_add_u32 %[sk], %[sk], 0x1000\n" \
    "s_add_u32 %[so], %[st], %[sk]\n" \
    "s_and_b32 %[so], %[so], 0xffff\n" \
    "s_and_b32 %[s2], %[sk], 0x10000\n" \
    "s_or_b32 %[so], %[so], %[s2]\n" \
    "buffer_load_dwordx2 v[144:145], %[vk], %[rs], %[so] offen offset:0 sc1\n" \
    "buffer_load_dwordx2 v[146:147], %[vk], %[rs], %[so] offen offset:512 sc1\n" \
    "buffer_load_dwordx2 v[148:149], %[vk], %[rs], %[so] offen offset:1024 sc1\n" \
    "buffer_load_dwordx2 v[150:151], %[vk], %[rs], %[so] offen offset:1536 sc1\n" \
    "buffer_load_dwordx4 v[152:155], %[vv], %[rs], %[so] offen offset:2048 sc1\n" \
    "buffer_load_dwordx4 v[156:159], %[vv], %[rs], %[so] offen offset:3072 sc1\n" \
    "s_add_u32 %[sk], %[sk], 0x1000\n" \
    "s_add_u32 %[so], %[st], %[sk]\n" \
    "s_and_b32 %[so], %[so], 0xffff\n" \
    "s_and_b32 %[s2], %[sk], 0x10000\n" \
    "s_or_b32 %[so], %[so], %[s2]\n" \
    "buffer_load_dwordx2 v[160:161], %[vk], %[rs], %[so] offen offset:0 sc1\n" \
    "buffer_load_dwordx2 v[162:163], %[vk], %[rs], %[so] offen offset:512 sc1\n" \
    "buffer_load_dwordx2 v[164:165], %[vk], %[rs], %[so] offen offset:1024 sc1\n" \
    "buffer_load_dwordx2 v[166:167], %[vk], %[rs], %[so] offen offset:1536 sc1\n" \
    "buffer_load_dwordx4 v[168:171], %[vv], %[rs], %[so] offen offset:2048 sc1\n" \
    "buffer_load_dwordx4 v[172:175], %[vv], %[rs], %[so] offen offset:3072 sc1\n" \
    "s_add_u32 %[sk], %[sk], 0x1000\n" \
    "s_add_u32 %[so], %[st], %[sk]\n" \
    "s_and_b32 %[so], %[so], 0xffff\n" \
    "s_and_b32 %[s2], %[sk], 0x10000\n" \
    "s_or_b32 %[so], %[so], %[s2]\n" \
    "s_mov_b32 exec_lo, 0\n" \
    "v_mov_b32 v38, 0\n" \
    "v_mov_b32 v39, 0\n" \
    "v_mov_b32 v46, 0\n" \
    "v_mov_b32 v47, 0\n" \
    "v_mov_b32 v54, 0\n" \
    "v_mov_b32 v55, 0\n" \
    "v_mov_b32 v62, 0\n" \
    "v_mov_b32 v63, 0\n" \
    "s_mov_b32 exec_lo, -1\n" \
    "v_mov_b32 v184, 0\n" \
    "v_mov_b32 v185, 0\n" \
    "v_mov_b32 v186, 0\n" \
    "v_mov_b32 v187, 0\n" \
    "v_mov_b32 v188, 0\n" \
    "v_mov_b32 v189, 0\n" \
    "v_mov_b32 v190, 0\n" \
    "v_mov_b32 v191, 0\n" \
    "v_mov_b32 v232, 0\n" \
    "v_mov_b32 v233, 0\n" \
    "v_mov_b32 v234, 0\n" \
    "v_mov_b32 v235, 0\n" \
    "v_mov_b32 v236, 0\n" \
    "v_mov_b32 v237, 0\n" \
    "v_mov_b32 v238, 0\n" \
    "v_mov_b32 v239, 0\n" \
    "v_mov_b32 v222, 0\n" \
    "v_mov_b32 v223, 0\n" \
    "v_mov_b32 v240, 0xff800000\n" \
    "v_mov_b32 v241, 0xff800000\n" \
    "v_mov_b32 v242, 0xff800000\n" \
    "v_mov_b32 v243, 0xff800000\n" \
    "s_cmp_eq_u32 %[md], 0\n" \
    "s_cselect_b32 %[cn], 1, 16\n" \
    "s_mov_b32 %[s2], %[st]\n" \
    "s_waitcnt vmcnt(12)\n" \
    "ATT_MAXL_%=:\n" \
    "v_mfma_f32_32x32x16_bf16 v[64:79], v[128:131], v[32:35], 0\n" \
    "v_mfma_f32_32x32x16_bf16 v[64:79], v[132:135], v[36:39], v[64:79]\n" \
    "v_mfma_f32_32x32x16_bf16 v[80:95], v[128:131], v[40:43], 0\n" \
    "v_mfma_f32_32x32x16_bf16 v[80:95], v[132:135], v[44:47], v[80:95]\n" \
    "v_mfma_f32_32x32x16_bf16 v[96:111], v[128:131], v[48:51], 0\n" \
    "v_mfma_f32_32x32x16_bf16 v[96:111], v[132:135], v[52:55], v[96:111]\n" \
    "v_mfma_f32_32x32x16_bf16 v[112:127], v[128:131], v[56:59], 0\n" \
    "v_mfma_f32_32x32x16_bf16 v[112:127], v[132:135], v[60:63], v[112:127]\n" \
    "s_nop 5\n" \
    "v_max3_f32 v240, v240, v64, v65\n" \
    "v_max3_f32 v240, v240, v66, v67\n" \
    "v_max3_f32 v240, v240, v68, v69\n" \
    "v_max3_f32 v240, v240, v70, v71\n" \
    "v_max3_f32 v240, v240, v72, v73\n" \
    "v_max3_f32 v240, v240, v74, v75\n" \
    "v_max3_f32 v240, v240, v76, v77\n" \
    "v_max3_f32 v240, v240, v78, v79\n" \
    "v_max3_f32 v241, v241, v80, v81\n" \
    "v_max3_f32 v241, v241, v82, v83\n" \
    "v_max3_f32 v241, v241, v84, v85\n" \
    "v_max3_f32 v241, v241, v86, v87\n" \
    "v_max3_f32 v241, v241, v88, v89\n" \
    "v_max3_f32 v241, v241, v90, v91\n" \
    "v_max3_f32 v241, v241, v92, v93\n" \
    "v_max3_f32 v241, v241, v94, v95\n" \
    "v_max3_f32 v242, v242, v96, v97\n" \
    "v_max3_f32 v242, v242, v98, v99\n" \
    "v_max3_f32 v242, v242, v100, v101\n" \
    "v_max3_f32 v242, v242, v102, v103\n" \
    "v_max3_f32 v242, v242, v104, v105\n" \
    "v_max3_f32 v242, v242, v106, v107\n" \
    "v_max3_f32 v242, v242, v108, v109\n" \
    "v_max3_f32 v242, v242, v110, v111\n" \
    "v_max3_f32 v243, v243, v112, v113\n" \
    "v_max3_f32 v243, v243, v114, v115\n" \
    "v_max3_f32 v243, v243, v116, v117\n" \
    "v_max3_f32 v243, v243, v118, v119\n" \
    "v_max3_f32 v243, v243, v120, v121\n" \
    "v_max3_f32 v243, v243, v122, v123\n" \
    "v_max3_f32 v243, v243, v124, v125\n" \
    "v_max3_f32 v243, v243, v126, v127\n" \
    "s_sub_u32 %[cn], %[cn], 1\n" \
    "s_cmp_eq_u32 %[cn], 0\n" \
    "s_cbranch_scc1 ATT_MAXD_%=\n" \
    "s_add_u32 %[s2], %[s2], 0x1000\n" \
    "s_and_b32 %[s2], %[s2], 0xffff\n" \
    "buffer_load_dwordx2 v[128:129], %[vk], %[rs], %[s2] offen offset:0 sc1\n" \
    "buffer_load_dwordx2 v[130:131], %[vk], %[rs], %[s2] offen offset:512 sc1\n" \
    "buffer_load_dwordx2 v[132:133], %[vk], %[rs], %[s2] offen offset:1024 sc1\n" \
    "buffer_load_dwordx2 v[134:135], %[vk], %[rs], %[s2] offen offset:1536 sc1\n" \
    "s_waitcnt vmcnt(0)\n" \
    "s_branch ATT_MAXL_%=\n" \
    "ATT_MAXD_%=:\n" \
    "s_cmp_eq_u32 %[md], 0\n" \
    "s_cbranch_scc1 ATT_NOREL_%=\n" \
    "buffer_load_dwordx2 v[128:129], %[vk], %[rs], %[st] offen offset:0 sc1\n" \
    "buffer_load_dwordx2 v[130:131], %[vk], %[rs], %[st] offen offset:512 sc1\n" \
    "buffer_load_dwordx2 v[132:133], %[vk], %[rs], %[st] offen offset:1024 sc1\n" \
    "buffer_load_dwordx2 v[134:135], %[vk], %[rs], %[st] offen offset:1536 sc1\n" \
    "s_waitcnt vmcnt(0)\n" \
    "ATT_NOREL_%=:\n" \
    "v_mov_b32 v244, v240\n" \
    "v_mov_b32 v245, v241\n" \
    "v_mov_b32 v246, v242\n" \
    "v_mov_b32 v247, v243\n" \
    "v_permlane32_swap_b32 v240, v244\n" \
    "v_permlane32_swap_b32 v241, v245\n" \
    "v_permlane32_swap_b32 v242, v246\n" \
    "v_permlane32_swap_b32 v243, v247\n" \
    "v_max_f32 v240, v240, v244\n" \
    "v_max_f32 v241, v241, v245\n" \
    "v_max_f32 v242, v242, v246\n" \
    "v_max_f32 v243, v243, v247\n" \
    "s_mov_b32 exec_lo, 0\n" \
    "v_cvt_pk_bf16_f32 v192, -v240, -v240\n" \
    "v_cvt_pk_bf16_f32 v193, -v241, -v241\n" \
    "v_cvt_pk_bf16_f32 v194, -v242, -v242\n" \
    "v_cvt_pk_bf16_f32 v195, -v243, -v243\n" \
    "v_lshlrev_b32 v192, 16, v192\n" \
    "v_lshlrev_b32 v193, 16, v193\n" \
    "v_lshlrev_b32 v194, 16, v194\n" \
    "v_lshlrev_b32 v195, 16, v195\n" \
    "v_sub_f32_e64 v196, -v240, v192\n" \
    "v_sub_f32_e64 v197, -v241, v193\n" \
    "v_sub_f32_e64 v198, -v242, v194\n" \
    "v_sub_f32_e64 v199, -v243, v195\n" \
    "v_cvt_pk_bf16_f32 v38, -v240, v196\n" \
    "v_cvt_pk_bf16_f32 v46, -v241, v197\n" \
    "v_cvt_pk_bf16_f32 v54, -v242, v198\n" \
    "v_cvt_pk_bf16_f32 v62, -v243, v199\n" \
    "v_and_b32 v192, 0xffff0000, v38\n" \
    "v_and_b32 v193, 0xffff0000, v46\n" \
    "v_and_b32 v194, 0xffff0000, v54\n" \
    "v_and_b32 v195, 0xffff0000, v62\n" \
    "v_sub_f32 v196, v196, v192\n" \
    "v_sub_f32 v197, v197, v193\n" \
    "v_sub_f32 v198, v198, v194\n" \
    "v_sub_f32 v199, v199, v195\n" \
    "v_cvt_pk_bf16_f32 v39, v196, 0\n" \
    "v_cvt_pk_bf16_f32 v47, v197, 0\n" \
    "v_cvt_pk_bf16_f32 v55, v198, 0\n" \
    "v_cvt_pk_bf16_f32 v63, v199, 0\n" \
    "s_mov_b32 exec_lo, -1\n" \
    "v_mfma_f32_32x32x16_bf16 v[192:207], v[128:131], v[32:35], 0\n" \
    "v_mfma_f32_32x32x16_bf16 v[192:207], v[132:135], v[36:39], v[192:207]\n" \
    "v_mfma_f32_32x32x16_bf16 v[64:79], v[184:187], v[184:187], 0\n" \
    "v_mfma_f32_32x32x16_bf16 v[80:95], v[184:187], v[184:187], 0\n" \
    "v_mfma_f32_32x32x16_bf16 v[96:111], v[184:187], v[184:187], 0\n" \
    "v_mfma_f32_32x32x16_bf16 v[112:127], v[184:187], v[184:187], 0\n" \
    "s_mov_b32 %[cn], 4\n" \
    "s_nop 4\n" \
    "ATT_LOOP_%=:\n" \
    "v_mfma_f32_32x32x16_bf16 v[112:127], v[184:187], v[232:235], v[112:127]\n" \
    "v_cvt_pk_bf16_f32 v239, v222, v223\n" \
    "v_exp_f32 v192, v192\n" \
    "v_exp_f32 v193, v193\n" \
    "v_exp_f32 v194, v194\n" \
    "v_cvt_pk_bf16_f32 v224, v192, v193\n" \
    "v_exp_f32 v195, v195\n" \
    "v_mfma_f32_32x32x16_bf16 v[208:223], v[128:131], v[40:43], 0\n" \
    "v_cvt_pk_bf16_f32 v225, v194, v195\n" \
    "v_exp_f32 v196, v196\n" \
    "v_exp_f32 v197, v197\n" \
    "v_exp_f32 v198, v198\n" \
    "v_cvt_pk_bf16_f32 v226, v196, v197\n" \
    "v_exp_f32 v199, v199\n" \
    "v_mfma_f32_32x32x16_bf16 v[208:223], v[132:135], v[44:47], v[208:223]\n" \
    "v_cvt_pk_bf16_f32 v227, v198, v199\n" \
    "v_exp_f32 v200, v200\n" \
    "v_exp_f32 v201, v201\n" \
    "v_exp_f32 v202, v202\n" \
    "v_cvt_pk_bf16_f32 v228, v200, v201\n" \
    "v_exp_f32 v203, v203\n" \
    "v_mfma_f32_32x32x16_bf16 v[112:127], v[188:191], v[236:239], v[112:127]\n" \
    "v_cvt_pk_bf16_f32 v229, v202, v203\n" \
    "v_exp_f32 v204, v204\n" \
    "v_exp_f32 v205, v205\n" \
    "v_exp_f32 v206, v206\n" \
    "v_cvt_pk_bf16_f32 v230, v204, v205\n" \
    "v_exp_f32 v207, v207\n" \
    "v_mfma_f32_32x32x16_bf16 v[64:79], v[136:139], v[224:227], v[64:79]\n" \
    "buffer_load_dwordx2 v[176:177], %[vk], %[rs], %[so] offen offset:0 sc1\n" \
    "buffer_load_dwordx2 v[178:179], %[vk], %[rs], %[so] offen offset:512 sc1\n" \
    "buffer_load_dwordx2 v[180:181], %[vk], %[rs], %[so] offen offset:1024 sc1\n" \
    "buffer_load_dwordx2 v[182:183], %[vk], %[rs], %[so] offen offset:1536 sc1\n" \
    "buffer_load_dwordx4 v[184:187], %[vv], %[rs], %[so] offen offset:2048 sc1\n" \
    "buffer_load_dwordx4 v[188:191], %[vv], %[rs], %[so] offen offset:3072 sc1\n" \
    "v_cvt_pk_bf16_f32 v231, v206, v207\n" \
    "v_exp_f32 v208, v208\n" \
    "v_exp_f32 v209, v209\n" \
    "v_exp_f32 v210, v210\n" \
    "v_cvt_pk_bf16_f32 v232, v208, v209\n" \
    "v_exp_f32 v211, v211\n" \
    "v_mfma_f32_32x32x16_bf16 v[192:207], v[128:131], v[48:51], 0\n" \
    "s_add_u32 %[sk], %[sk], 0x1000\n" \
    "v_cvt_pk_bf16_f32 v233, v210, v211\n" \
    "v_exp_f32 v212, v212\n" \
    "v_exp_f32 v213, v213\n" \
    "v_exp_f32 v214, v214\n" \
    "v_cvt_pk_bf16_f32 v234, v212, v213\n" \
    "v_exp_f32 v215, v215\n" \
    "v_mfma_f32_32x32x16_bf16 v[192:207], v[132:135], v[52:55], v[192:207]\n" \
    "s_add_u32 %[so], %[st], %[sk]\n" \
    "v_cvt_pk_bf16_f32 v235, v214, v215\n" \
    "v_exp_f32 v216, v216\n" \
    "v_exp_f32 v217, v217\n" \
    "v_exp_f32 v218, v218\n" \
    "v_cvt_pk_bf16_f32 v236, v216, v217\n" \
    "v_exp_f32 v219, v219\n" \
    "v_mfma_f32_32x32x16_bf16 v[64:79], v[140:143], v[228:231], v[64:79]\n" \
    "s_and_b32 %[so], %[so], 0xffff\n" \
    "v_cvt_pk_bf16_f32 v237, v218, v219\n" \
    "v_exp_f32 v220, v220\n" \
    "v_exp_f32 v221, v221\n" \
    "v_exp_f32 v222, v222\n" \
    "v_cvt_pk_bf16_f32 v238, v220, v221\n" \
    "v_exp_f32 v223, v223\n" \
    "v_mfma_f32_32x32x16_bf16 v[80:95], v[136:139], v[232:235], v[80:95]\n" \
    "s_and_b32 %[s2], %[sk], 0x10000\n" \
    "v_cvt_pk_bf16_f32 v239, v222, v223\n" \
    "v_exp_f32 v192, v192\n" \
    "v_exp_f32 v193, v193\n" \
    "v_exp_f32 v194, v194\n" \
    "v_cvt_pk_bf16_f32 v224, v192, v193\n" \
    "v_exp_f32 v195, v195\n" \
    "v_mfma_f32_32x32x16_bf16 v[208:223], v[128:131], v[56:59], 0\n" \
    "s_or_b32 %[so], %[so], %[s2]\n" \
    "v_cvt_pk_bf16_f32 v225, v194, v195\n" \
    "v_exp_f32 v196, v196\n" \
    "v_exp_f32 v197, v197\n" \
    "v_exp_f32 v198, v198\n" \
    "v_cvt_pk_bf16_f32 v226, v196, v197\n" \
    "v_exp_f32 v199, v199\n" \
    "v_mfma_f32_32x32x16_bf16 v[208:223], v[132:135], v[60:63], v[208:223]\n" \
    "v_cvt_pk_bf16_f32 v227, v198, v199\n" \
    "v_exp_f32 v200, v200\n" \
    "v_exp_f32 v201, v201\n" \
    "v_exp_f32 v202, v202\n" \
    "v_cvt_pk_bf16_f32 v228, v200, v201\n" \
    "v_exp_f32 v203, v203\n" \
    "v_mfma_f32_32x32x16_bf16 v[80:95], v[140:143], v[236:239], v[80:95]\n" \
    "v_cvt_pk_bf16_f32 v229, v202, v203\n" \
    "v_exp_f32 v204, v204\n" \
    "v_exp_f32 v205, v205\n" \
    "v_exp_f32 v206, v206\n" \
    "v_cvt_pk_bf16_f32 v230, v204, v205\n" \
    "v_exp_f32 v207, v207\n" \
    "v_mfma_f32_32x32x16_bf16 v[96:111], v[136:139], v[224:227], v[96:111]\n" \
    "s_waitcnt vmcnt(12)\n" \
    "v_cvt_pk_bf16_f32 v231, v206, v207\n" \
    "v_exp_f32 v208, v208\n" \
    "v_exp_f32 v209, v209\n" \
    "v_exp_f32 v210, v210\n" \
    "v_cvt_pk_bf16_f32 v232, v208, v209\n" \
    "v_exp_f32 v211, v211\n" \
    "v_mfma_f32_32x32x16_bf16 v[192:207], v[144:147], v[32:35], 0\n" \
    "v_cvt_pk_bf16_f32 v233, v210, v211\n" \
    "v_exp_f32 v212, v212\n" \
    "v_exp_f32 v213, v213\n" \
    "v_exp_f32 v214, v214\n" \
    "v_cvt_pk_bf16_f32 v234, v212, v213\n" \
    "v_exp_f32 v215, v215\n" \
    "v_mfma_f32_32x32x16_bf16 v[192:207], v[148:151], v[36:39], v[192:207]\n" \
    "v_cvt_pk_bf16_f32 v235, v214, v215\n" \
    "v_exp_f32 v216, v216\n" \
    "v_exp_f32 v217, v217\n" \
    "v_exp_f32 v218, v218\n" \
    "v_cvt_pk_bf16_f32 v236, v216, v217\n" \
    "v_exp_f32 v219, v219\n" \
    "v_mfma_f32_32x32x16_bf16 v[96:111], v[140:143], v[228:231], v[96:111]\n" \
    "v_cvt_pk_bf16_f32 v237, v218, v219\n" \
    "v_exp_f32 v220, v220\n" \
    "v_exp_f32 v221, v221\n" \
    "v_exp_f32 v222, v222\n" \
    "v_cvt_pk_bf16_f32 v238, v220, v221\n" \
    "v_exp_f32 v223, v223\n" \
    "v_mfma_f32_32x32x16_bf16 v[112:127], v[136:139], v[232:235], v[112:127]\n" \
    "v_cvt_pk_bf16_f32 v239, v222, v223\n" \
    "v_exp_f32 v192, v192\n" \
    "v_exp_f32 v193, v193\n" \
    "v_exp_f32 v194, v194\n" \
    "v_cvt_pk_bf16_f32 v224, v192, v193\n" \
    "v_exp_f32 v195, v195\n" \
    "v_mfma_f32_32x32x16_bf16 v[208:223], v[144:147], v[40:43], 0\n" \
    "v_cvt_pk_bf16_f32 v225, v194, v195\n" \
    "v_exp_f32 v196, v196\n" \
    "v_exp_f32 v197, v197\n" \
    "v_exp_f32 v198, v198\n" \
    "v_cvt_pk_bf16_f32 v226, v196, v197\n" \
    "v_exp_f32 v199, v199\n" \
    "v_mfma_f32_32x32x16_bf16 v[208:223], v[148:151], v[44:47], v[208:223]\n" \
    "v_cvt_pk_bf16_f32 v227, v198, v199\n" \
    "v_exp_f32 v200, v200\n" \
    "v_exp_f32 v201, v201\n" \
    "v_exp_f32 v202, v202\n" \
    "v_cvt_pk_bf16_f32 v228, v200, v201\n" \
    "v_exp_f32 v203, v203\n" \
    "v_mfma_f32_32x32x16_bf16 v[112:127], v[140:143], v[236:239], v[112:127]\n" \
    "v_cvt_pk_bf16_f32 v229, v202, v203\n" \
    "v_exp_f32 v204, v204\n" \
    "v_exp_f32 v205, v205\n" \
    "v_exp_f32 v206, v206\n" \
    "v_cvt_pk_bf16_f32 v230, v204, v205\n" \
    "v_exp_f32 v207, v207\n" \
    "v_mfma_f32_32x32x16_bf16 v[64:79], v[152:155], v[224:227], v[64:79]\n" \
    "buffer_load_dwordx2 v[128:129], %[vk], %[rs], %[so] offen offset:0 sc1\n" \
    "buffer_load_dwordx2 v[130:131], %[vk], %[rs], %[so] offen offset:512 sc1\n" \
    "buffer_load_dwordx2 v[132:133], %[vk], %[rs], %[so] offen offset:1024 sc1\n" \
    "buffer_load_dwordx2 v[134:135], %[vk], %[rs], %[so] offen offset:1536 sc1\n" \
    "buffer_load_dwordx4 v[136:139], %[vv], %[rs], %[so] offen offset:2048 sc1\n" \
    "buffer_load_dwordx4 v[140:143], %[vv], %[rs], %[so] offen offset:3072 sc1\n" \
    "v_cvt_pk_bf16_f32 v231, v206, v207\n" \
    "v_exp_f32 v208, v208\n" \
    "v_exp_f32 v209, v209\n" \
    "v_exp_f32 v210, v210\n" \
    "v_cvt_pk_bf16_f32 v232, v208, v209\n" \
    "v_exp_f32 v211, v211\n" \
    "v_mfma_f32_32x32x16_bf16 v[192:207], v[144:147], v[48:51], 0\n" \
    "s_add_u32 %[sk], %[sk], 0x1000\n" \
    "v_cvt_pk_bf16_f32 v233, v210, v211\n" \
    "v_exp_f32 v212, v212\n" \
    "v_exp_f32 v213, v213\n" \
    "v_exp_f32 v214, v214\n" \
    "v_cvt_pk_bf16_f32 v234, v212, v213\n" \
    "v_exp_f32 v215, v215\n" \
    "v_mfma_f32_32x32x16_bf16 v[192:207], v[148:151], v[52:55], v[192:207]\n" \
    "s_add_u32 %[so], %[st], %[sk]\n" \
    "v_cvt_pk_bf16_f32 v235, v214, v215\n" \
    "v_exp_f32 v216, v216\n" \
    "v_exp_f32 v217, v217\n" \
    "v_exp_f32 v218, v218\n" \
    "v_cvt_pk_bf16_f32 v236, v216, v217\n" \
    "v_exp_f32 v219, v219\n" \
    "v_mfma_f32_32x32x16_bf16 v[64:79], v[156:159], v[228:231], v[64:79]\n" \
    "s_and_b32 %[so], %[so], 0xffff\n" \
    "v_cvt_pk_bf16_f32 v237, v218, v219\n" \
    "v_exp_f32 v220, v220\n" \
    "v_exp_f32 v221, v221\n" \
    "v_exp_f32 v222, v222\n" \
    "v_cvt_pk_bf16_f32 v238, v220, v221\n" \
    "v_exp_f32 v223, v223\n" \
    "v_mfma_f32_32x32x16_bf16 v[80:95], v[152:155], v[232:235], v[80:95]\n" \
    "s_and_b32 %[s2], %[sk], 0x10000\n" \
    "v_cvt_pk_bf16_f32 v239, v222, v223\n" \
    "v_exp_f32 v192, v192\n" \
    "v_exp_f32 v193, v193\n" \
    "v_exp_f32 v194, v194\n" \
    "v_cvt_pk_bf16_f32 v224, v192, v193\n" \
    "v_exp_f32 v195, v195\n" \
    "v_mfma_f32_32x32x16_bf16 v[208:223], v[144:147], v[56:59], 0\n" \
    "s_or_b32 %[so], %[so], %[s2]\n" \
    "v_cvt_pk_bf16_f32 v225, v194, v195\n" \
    "v_exp_f32 v196, v196\n" \
    "v_exp_f32 v197, v197\n" \
    "v_exp_f32 v198, v198\n" \
    "v_cvt_pk_bf16_f32 v226, v196, v197\n" \
    "v_exp_f32 v199, v199\n" \
    "v_mfma_f32_32x32x16_bf16 v[208:223], v[148:151], v[60:63], v[208:223]\n" \
    "v_cvt_pk_bf16_f32 v227, v198, v199\n" \
    "v_exp_f32 v200, v200\n" \
    "v_exp_f32 v201, v201\n" \
    "v_exp_f32 v202, v202\n" \
    "v_cvt_pk_bf16_f32 v228, v200, v201\n" \
    "v_exp_f32 v203, v203\n" \
    "v_mfma_f32_32x32x16_bf16 v[80:95], v[156:159], v[236:239], v[80:95]\n" \
    "v_cvt_pk_bf16_f32 v229, v202, v203\n" \
    "v_exp_f32 v204, v204\n" \
    "v_exp_f32 v205, v205\n" \
    "v_exp_f32 v206, v206\n" \
    "v_cvt_pk_bf16_f32 v230, v204, v205\n" \
    "v_exp_f32 v207, v207\n" \
    "v_mfma_f32_32x32x16_bf16 v[96:111], v[152:155], v[224:227], v[96:111]\n" \
    "s_waitcnt vmcnt(12)\n" \
    "v_cvt_pk_bf16_f32 v231, v206, v207\n" \
    "v_exp_f32 v208, v208\n" \
    "v_exp_f32 v209, v209\n" \
    "v_exp_f32 v210, v210\n" \
    "v_cvt_pk_bf16_f32 v232, v208, v209\n" \
    "v_exp_f32 v211, v211\n" \
    "v_mfma_f32_32x32x16_bf16 v[192:207], v[160:163], v[32:35], 0\n" \
    "v_cvt_pk_bf16_f32 v233, v210, v211\n" \
    "v_exp_f32 v212, v212\n" \
    "v_exp_f32 v213, v213\n" \
    "v_exp_f32 v214, v214\n" \
    "v_cvt_pk_bf16_f32 v234, v212, v213\n" \
    "v_exp_f32 v215, v215\n" \
    "v_mfma_f32_32x32x16_bf16 v[192:207], v[164:167], v[36:39], v[192:207]\n" \
    "v_cvt_pk_bf16_f32 v235, v214, v215\n" \
    "v_exp_f32 v216, v216\n" \
    "v_exp_f32 v217, v217\n" \
    "v_exp_f32 v218, v218\n" \
    "v_cvt_pk_bf16_f32 v236, v216, v217\n" \
    "v_exp_f32 v219, v219\n" \
    "v_mfma_f32_32x32x16_bf16 v[96:111], v[156:159], v[228:231], v[96:111]\n" \
    "v_cvt_pk_bf16_f32 v237, v218, v219\n" \
    "v_exp_f32 v220, v220\n" \
    "v_exp_f32 v221, v221\n" \
    "v_exp_f32 v222, v222\n" \
    "v_cvt_pk_bf16_f32 v238, v220, v221\n" \
    "v_exp_f32 v223, v223\n" \
    "v_mfma_f32_32x32x16_bf16 v[112:127], v[152:155], v[232:235], v[112:127]\n" \
    "v_cvt_pk_bf16_f32 v239, v222, v223\n" \
    "v_exp_f32 v192, v192\n" \
    "v_exp_f32 v193, v193\n" \
    "v_exp_f32 v194, v194\n" \
    "v_cvt_pk_bf16_f32 v224, v192, v193\n" \
    "v_exp_f32 v195, v195\n" \
    "v_mfma_f32_32x32x16_bf16 v[208:223], v[160:163], v[40:43], 0\n" \
    "v_cvt_pk_bf16_f32 v225, v194, v195\n" \
    "v_exp_f32 v196, v196\n" \
    "v_exp_f32 v197, v197\n" \
    "v_exp_f32 v198, v198\n" \
    "v_cvt_pk_bf16_f32 v226, v196, v197\n" \
    "v_exp_f32 v199, v199\n" \
    "v_mfma_f32_32x32x16_bf16 v[208:223], v[164:167], v[44:47], v[208:223]\n" \
    "v_cvt_pk_bf16_f32 v227, v198, v199\n" \
    "v_exp_f32 v200, v200\n" \
    "v_exp_f32 v201, v201\n" \
    "v_exp_f32 v202, v202\n" \
    "v_cvt_pk_bf16_f32 v228, v200, v201\n" \
    "v_exp_f32 v203, v203\n" \
    "v_mfma_f32_32x32x16_bf16 v[112:127], v[156:159], v[236:239], v[112:127]\n" \
    "v_cvt_pk_bf16_f32 v229, v202, v203\n" \
    "v_exp_f32 v204, v204\n" \
    "v_exp_f32 v205, v205\n" \
    "v_exp_f32 v206, v206\n" \
    "v_cvt_pk_bf16_f32 v230, v204, v205\n" \
    "v_exp_f32 v207, v207\n" \
    "v_mfma_f32_32x32x16_bf16 v[64:79], v[168:171], v[224:227], v[64:79]\n" \
    "buffer_load_dwordx2 v[144:145], %[vk], %[rs], %[so] offen offset:0 sc1\n" \
    "buffer_load_dwordx2 v[146:147], %[vk], %[rs], %[so] offen offset:512 sc1\n" \
    "buffer_load_dwordx2 v[148:149], %[vk], %[rs], %[so] offen offset:1024 sc1\n" \
    "buffer_load_dwordx2 v[150:151], %[vk], %[rs], %[so] offen offset:1536 sc1\n" \
    "buffer_load_dwordx4 v[152:155], %[vv], %[rs], %[so] offen offset:2048 sc1\n" \
    "buffer_load_dwordx4 v[156:159], %[vv], %[rs], %[so] offen offset:3072 sc1\n" \
    "v_cvt_pk_bf16_f32 v231, v206, v207\n" \
    "v_exp_f32 v208, v208\n" \
    "v_exp_f32 v209, v209\n" \
    "v_exp_f32 v210, v210\n" \
    "v_cvt_pk_bf16_f32 v232, v208, v209\n" \
    "v_exp_f32 v211, v211\n" \
    "v_mfma_f32_32x32x16_bf16 v[192:207], v[160:163], v[48:51], 0\n" \
    "s_add_u32 %[sk], %[sk], 0x1000\n" \
    "v_cvt_pk_bf16_f32 v233, v210, v211\n" \
    "v_exp_f32 v212, v212\n" \
    "v_exp_f32 v213, v213\n" \
    "v_exp_f32 v214, v214\n" \
    "v_cvt_pk_bf16_f32 v234, v212, v213\n" \
    "v_exp_f32 v215, v215\n" \
    "v_mfma_f32_32x32x16_bf16 v[192:207], v[164:167], v[52:55], v[192:207]\n" \
    "s_add_u32 %[so], %[st], %[sk]\n" \
    "v_cvt_pk_bf16_f32 v235, v214, v215\n" \
    "v_exp_f32 v216, v216\n" \
    "v_exp_f32 v217, v217\n" \
    "v_exp_f32 v218, v218\n" \
    "v_cvt_pk_bf16_f32 v236, v216, v217\n" \
    "v_exp_f32 v219, v219\n" \
    "v_mfma_f32_32x32x16_bf16 v[64:79], v[172:175], v[228:231], v[64:79]\n" \
    "s_and_b32 %[so], %[so], 0xffff\n" \
    "v_cvt_pk_bf16_f32 v237, v218, v219\n" \
    "v_exp_f32 v220, v220\n" \
    "v_exp_f32 v221, v221\n" \
    "v_exp_f32 v222, v222\n" \
    "v_cvt_pk_bf16_f32 v238, v220, v221\n" \
    "v_exp_f32 v223, v223\n" \
    "v_mfma_f32_32x32x16_bf16 v[80:95], v[168:171], v[232:235], v[80:95]\n" \
    "s_and_b32 %[s2], %[sk], 0x10000\n" \
    "v_cvt_pk_bf16_f32 v239, v222, v223\n" \
    "v_exp_f32 v192, v192\n" \
    "v_exp_f32 v193, v193\n" \
    "v_exp_f32 v194, v194\n" \
    "v_cvt_pk_bf16_f32 v224, v192, v193\n" \
    "v_exp_f32 v195, v195\n" \
    "v_mfma_f32_32x32x16_bf16 v[208:223], v[160:163], v[56:59], 0\n" \
    "s_or_b32 %[so], %[so], %[s2]\n" \
    "v_cvt_pk_bf16_f32 v225, v194, v195\n" \
    "v_exp_f32 v196, v196\n" \
    "v_exp_f32 v197, v197\n" \
    "v_exp_f32 v198, v198\n" \
    "v_cvt_pk_bf16_f32 v226, v196, v197\n" \
    "v_exp_f32 v199, v199\n" \
    "v_mfma_f32_32x32x16_bf16 v[208:223], v[164:167], v[60:63], v[208:223]\n" \
    "v_cvt_pk_bf16_f32 v227, v198, v199\n" \
    "v_exp_f32 v200, v200\n" \
    "v_exp_f32 v201, v201\n" \
    "v_exp_f32 v202, v202\n" \
    "v_cvt_pk_bf16_f32 v228, v200, v201\n" \
    "v_exp_f32 v203, v203\n" \
    "v_mfma_f32_32x32x16_bf16 v[80:95], v[172:175], v[236:239], v[80:95]\n" \
    "v_cvt_pk_bf16_f32 v229, v202, v203\n" \
    "v_exp_f32 v204, v204\n" \
    "v_exp_f32 v205, v205\n" \
    "v_exp_f32 v206, v206\n" \
    "v_cvt_pk_bf16_f32 v230, v204, v205\n" \
    "v_exp_f32 v207, v207\n" \
    "v_mfma_f32_32x32x16_bf16 v[96:111], v[168:171], v[224:227], v[96:111]\n" \
    "s_waitcnt vmcnt(12)\n" \
    "v_cvt_pk_bf16_f32 v231, v206, v207\n" \
    "v_exp_f32 v208, v208\n" \
    "v_exp_f32 v209, v209\n" \
    "v_exp_f32 v210, v210\n" \
    "v_cvt_pk_bf16_f32 v232, v208, v209\n" \
    "v_exp_f32 v211, v211\n" \
    "v_mfma_f32_32x32x16_bf16 v[192:207], v[176:179], v[32:35], 0\n" \
    "v_cvt_pk_bf16_f32 v233, v210, v211\n" \
    "v_exp_f32 v212, v212\n" \
    "v_exp_f32 v213, v213\n" \
    "v_exp_f32 v214, v214\n" \
    "v_cvt_pk_bf16_f32 v234, v212, v213\n" \
    "v_exp_f32 v215, v215\n" \
    "v_mfma_f32_32x32x16_bf16 v[192:207], v[180:183], v[36:39], v[192:207]\n" \
    "v_cvt_pk_bf16_f32 v235, v214, v215\n" \
    "v_exp_f32 v216, v216\n" \
    "v_exp_f32 v217, v217\n" \
    "v_exp_f32 v218, v218\n" \
    "v_cvt_pk_bf16_f32 v236, v216, v217\n" \
    "v_exp_f32 v219, v219\n" \
    "v_mfma_f32_32x32x16_bf16 v[96:111], v[172:175], v[228:231], v[96:111]\n" \
    "v_cvt_pk_bf16_f32 v237, v218, v219\n" \
    "v_exp_f32 v220, v220\n" \
    "v_exp_f32 v221, v221\n" \
    "v_exp_f32 v222, v222\n" \
    "v_cvt_pk_bf16_f32 v238, v220, v221\n" \
    "v_exp_f32 v223, v223\n" \
    "v_mfma_f32_32x32x16_bf16 v[112:127], v[168:171], v[232:235], v[112:127]\n" \
    "v_cvt_pk_bf16_f32 v239, v222, v223\n" \
    "v_exp_f32 v192, v192\n" \
    "v_exp_f32 v193, v193\n" \
    "v_exp_f32 v194, v194\n" \
    "v_cvt_pk_bf16_f32 v224, v192, v193\n" \
    "v_exp_f32 v195, v195\n" \
    "v_mfma_f32_32x32x16_bf16 v[208:223], v[176:179], v[40:43], 0\n" \
    "v_cvt_pk_bf16_f32 v225, v194, v195\n" \
    "v_exp_f32 v196, v196\n" \
    "v_exp_f32 v197, v197\n" \
    "v_exp_f32 v198, v198\n" \
    "v_cvt_pk_bf16_f32 v226, v196, v197\n" \
    "v_exp_f32 v199, v199\n" \
    "v_mfma_f32_32x32x16_bf16 v[208:223], v[180:183], v[44:47], v[208:223]\n" \
    "v_cvt_pk_bf16_f32 v227, v198, v199\n" \
    "v_exp_f32 v200, v200\n" \
    "v_exp_f32 v201, v201\n" \
    "v_exp_f32 v202, v202\n" \
    "v_cvt_pk_bf16_f32 v228, v200, v201\n" \
    "v_exp_f32 v203, v203\n" \
    "v_mfma_f32_32x32x16_bf16 v[112:127], v[172:175], v[236:239], v[112:127]\n" \
    "v_cvt_pk_bf16_f32 v229, v202, v203\n" \
    "v_exp_f32 v204, v204\n" \
    "v_exp_f32 v205, v205\n" \
    "v_exp_f32 v206, v206\n" \
    "v_cvt_pk_bf16_f32 v230, v204, v205\n" \
    "v_exp_f32 v207, v207\n" \
    "v_mfma_f32_32x32x16_bf16 v[64:79], v[184:187], v[224:227], v[64:79]\n" \
    "buffer_load_dwordx2 v[160:161], %[vk], %[rs], %[so] offen offset:0 sc1\n" \
    "buffer_load_dwordx2 v[162:163], %[vk], %[rs], %[so] offen offset:512 sc1\n" \
    "buffer_load_dwordx2 v[164:165], %[vk], %[rs], %[so] offen offset:1024 sc1\n" \
    "buffer_load_dwordx2 v[166:167], %[vk], %[rs], %[so] offen offset:1536 sc1\n" \
    "buffer_load_dwordx4 v[168:171], %[vv], %[rs], %[so] offen offset:2048 sc1\n" \
    "buffer_load_dwordx4 v[172:175], %[vv], %[rs], %[so] offen offset:3072 sc1\n" \
    "v_cvt_pk_bf16_f32 v231, v206, v207\n" \
    "v_exp_f32 v208, v208\n" \
    "v_exp_f32 v209, v209\n" \
    "v_exp_f32 v210, v210\n" \
    "v_cvt_pk_bf16_f32 v232, v208, v209\n" \
    "v_exp_f32 v211, v211\n" \
    "v_mfma_f32_32x32x16_bf16 v[192:207], v[176:179], v[48:51], 0\n" \
    "s_add_u32 %[sk], %[sk], 0x1000\n" \
    "v_cvt_pk_bf16_f32 v233, v210, v211\n" \
    "v_exp_f32 v212, v212\n" \
    "v_exp_f32 v213, v213\n" \
    "v_exp_f32 v214, v214\n" \
    "v_cvt_pk_bf16_f32 v234, v212, v213\n" \
    "v_exp_f32 v215, v215\n" \
    "v_mfma_f32_32x32x16_bf16 v[192:207], v[180:183], v[52:55], v[192:207]\n" \
    "s_add_u32 %[so], %[st], %[sk]\n" \
    "v_cvt_pk_bf16_f32 v235, v214, v215\n" \
    "v_exp_f32 v216, v216\n" \
    "v_exp_f32 v217, v217\n" \
    "v_exp_f32 v218, v218\n" \
    "v_cvt_pk_bf16_f32 v236, v216, v217\n" \
    "v_exp_f32 v219, v219\n" \
    "v_mfma_f32_32x32x16_bf16 v[64:79], v[188:191], v[228:231], v[64:79]\n" \
    "s_and_b32 %[so], %[so], 0xffff\n" \
    "v_cvt_pk_bf16_f32 v237, v218, v219\n" \
    "v_exp_f32 v220, v220\n" \
    "v_exp_f32 v221, v221\n" \
    "v_exp_f32 v222, v222\n" \
    "v_cvt_pk_bf16_f32 v238, v220, v221\n" \
    "v_exp_f32 v223, v223\n" \
    "v_mfma_f32_32x32x16_bf16 v[80:95], v[184:187], v[232:235], v[80:95]\n" \
    "s_and_b32 %[s2], %[sk], 0x10000\n" \
    "v_cvt_pk_bf16_f32 v239, v222, v223\n" \
    "v_exp_f32 v192, v192\n" \
    "v_exp_f32 v193, v193\n" \
    "v_exp_f32 v194, v194\n" \
    "v_cvt_pk_bf16_f32 v224, v192, v193\n" \
    "v_exp_f32 v195, v195\n" \
    "v_mfma_f32_32x32x16_bf16 v[208:223], v[176:179], v[56:59], 0\n" \
    "s_or_b32 %[so], %[so], %[s2]\n" \
    "v_cvt_pk_bf16_f32 v225, v194, v195\n" \
    "v_exp_f32 v196, v196\n" \
    "v_exp_f32 v197, v197\n" \
    "v_exp_f32 v198, v198\n" \
    "v_cvt_pk_bf16_f32 v226, v196, v197\n" \
    "v_exp_f32 v199, v199\n" \
    "v_mfma_f32_32x32x16_bf16 v[208:223], v[180:183], v[60:63], v[208:223]\n" \
    "v_cvt_pk_bf16_f32 v227, v198, v199\n" \
    "v_exp_f32 v200, v200\n" \
    "v_exp_f32 v201, v201\n" \
    "v_exp_f32 v202, v202\n" \
    "v_cvt_pk_bf16_f32 v228, v200, v201\n" \
    "v_exp_f32 v203, v203\n" \
    "v_mfma_f32_32x32x16_bf16 v[80:95], v[188:191], v[236:239], v[80:95]\n" \
    "v_cvt_pk_bf16_f32 v229, v202, v203\n" \
    "v_exp_f32 v204, v204\n" \
    "v_exp_f32 v205, v205\n" \
    "v_exp_f32 v206, v206\n" \
    "v_cvt_pk_bf16_f32 v230, v204, v205\n" \
    "v_exp_f32 v207, v207\n" \
    "v_mfma_f32_32x32x16_bf16 v[96:111], v[184:187], v[224:227], v[96:111]\n" \
    "s_waitcnt vmcnt(12)\n" \
    "v_cvt_pk_bf16_f32 v231, v206, v207\n" \
    "v_exp_f32 v208, v208\n" \
    "v_exp_f32 v209, v209\n" \
    "v_exp_f32 v210, v210\n" \
    "v_cvt_pk_bf16_f32 v232, v208, v209\n" \
    "v_exp_f32 v211, v211\n" \
    "v_mfma_f32_32x32x16_bf16 v[192:207], v[128:131], v[32:35], 0\n" \
    "v_cvt_pk_bf16_f32 v233, v210, v211\n" \
    "v_exp_f32 v212, v212\n" \
    "v_exp_f32 v213, v213\n" \
    "v_exp_f32 v214, v214\n" \
    "v_cvt_pk_bf16_f32 v234, v212, v213\n" \
    "v_exp_f32 v215, v215\n" \
    "v_mfma_f32_32x32x16_bf16 v[192:207], v[132:135], v[36:39], v[192:207]\n" \
    "s_sub_u32 %[cn], %[cn], 1\n" \
    "v_cvt_pk_bf16_f32 v235, v214, v215\n" \
    "v_exp_f32 v216, v216\n" \
    "v_exp_f32 v217, v217\n" \
    "v_exp_f32 v218, v218\n" \
    "v_cvt_pk_bf16_f32 v236, v216, v217\n" \
    "v_exp_f32 v219, v219\n" \
    "v_mfma_f32_32x32x16_bf16 v[96:111], v[188:191], v[228:231], v[96:111]\n" \
    "s_cmp_lg_u32 %[cn], 0\n" \
    "v_cvt_pk_bf16_f32 v237, v218, v219\n" \
    "v_exp_f32 v220, v220\n" \
    "v_exp_f32 v221, v221\n" \
    "v_exp_f32 v222, v222\n" \
    "v_cvt_pk_bf16_f32 v238, v220, v221\n" \
    "v_exp_f32 v223, v223\n" \
    "s_cbranch_scc1 ATT_LOOP_%=\n" \
    "v_mfma_f32_32x32x16_bf16 v[112:127], v[184:187], v[232:235], v[112:127]\n" \
    "v_cvt_pk_bf16_f32 v239, v222, v223\n" \
    "s_nop 1\n" \
    "v_mfma_f32_32x32x16_bf16 v[112:127], v[188:191], v[236:239], v[112:127]\n" \
    "s_waitcnt vmcnt(0)\n" \
    "s_nop 7\n" \
    "s_nop 3\n" \
    ""
#define ATTN_CLOB "v128", "v129", "v130", "v131", "v132", "v133", "v134", "v135", "v136", "v137", "v138", "v139", "v140", "v141", "v142", "v143", "v144", "v145", "v146", "v147", "v148", "v149", "v150", "v151", "v152", "v153", "v154", "v155", "v156", "v157", "v158", "v159", "v160", "v161", "v162", "v163", "v164", "v165", "v166", "v167", "v168", "v169", "v170", "v171", "v172", "v173", "v174", "v175", "v176", "v177", "v178", "v179", "v180", "v181", "v182", "v183", "v184", "v185", "v186", "v187", "v188", "v189", "v190", "v191", "v192", "v193", "v194", "v195", "v196", "v197", "v198", "v199", "v200", "v201", "v202", "v203", "v204", "v205", "v206", "v207", "v208", "v209", "v210", "v211", "v212", "v213", "v214", "v215", "v216", "v217", "v218", "v219", "v220", "v221", "v222", "v223", "v224", "v225", "v226", "v227", "v228", "v229", "v230", "v231", "v232", "v233", "v234", "v235", "v236", "v237", "v238", "v239", "v244", "v245", "v246", "v247"

DI rsrc_t mkrs(const void* p, unsigned bytes) { return __builtin_amdgcn_make_buffer_rsrc((void*)p, (short)0, (int)bytes, 0x00020000); }
DI void dma1(unsigned m0v, unsigned vo, rsrc_t rs, unsigned so) {
    asm volatile("s_mov_b32 m0, %0\n\ts_nop 0\n\tbuffer_load_dwordx4 %1, %2, %3 offen lds" ::"s"(m0v), "v"(vo), "s"(rs), "s"(so) : "memory");
}
DI void dma1c(unsigned m0v, unsigned vo, rsrc_t rs, unsigned so) {
    asm volatile("s_mov_b32 m0, %0\n\ts_nop 0\n\tbuffer_load_dwordx4 %1, %2, %3 offen sc1 lds" ::"s"(m0v), "v"(vo), "s"(rs), "s"(so) : "memory");
}
DI void dma2(unsigned m0v, unsigned v0, unsigned v1, rsrc_t rs, unsigned so) {
    asm volatile("s_mov_b32 m0, %0\n\ts_nop 0\n\tbuffer_load_dwordx4 %1, %3, %4 offen lds\n\tbuffer_load_dwordx4 %2, %3, %4 offen offset:1024 lds"
                 ::"s"(m0v), "v"(v0), "v"(v1), "s"(rs), "s"(so) : "memory");
}
DI void dma4(unsigned m0v, unsigned v0, unsigned v1, unsigned v2, unsigned v3, rsrc_t rs, unsigned so) {
    asm volatile("s_mov_b32 m0, %0\n\ts_nop 0\n\tbuffer_load_dwordx4 %1, %5, %6 offen lds\n\tbuffer_load_dwordx4 %2, %5, %6 offen offset:1024 lds\n\t"
                 "buffer_load_dwordx4 %3, %5, %6 offen offset:2048 lds\n\tbuffer_load_dwordx4 %4, %5, %6 offen offset:3072 lds"
                 ::"s"(m0v), "v"(v0), "v"(v1), "v"(v2), "v"(v3), "s"(rs), "s"(so) : "memory");
}
#define WAITV(n) asm volatile("s_waitcnt vmcnt(" #n ")" ::: "memory")
#define BAR() asm volatile("s_waitcnt lgkmcnt(0)\n\ts_barrier" ::: "memory")
#define RLX __ATOMIC_RELAXED, __HIP_MEMORY_SCOPE_AGENT
template <class T> DI T ldsr(unsigned a) { return *(const LAS T*)a; }
template <class T> DI void ldsw(unsigned a, T v) { *(LAS T*)a = v; }
DI s16x4 trr(unsigned a) { return __builtin_amdgcn_ds_read_tr16_b64_v4i16((LAS s16x4*)a); }
DI bf16x8 tr8(unsigned a0, unsigned a1) { s16x4 lo = trr(a0), hi = trr(a1); return __builtin_bit_cast(bf16x8, __builtin_shufflevector(lo, hi, 0, 1, 2, 3, 4, 5, 6, 7)); }
DI unsigned pk(float lo, float hi) { f32x2 v = {lo, hi}; return __builtin_bit_cast(unsigned, __builtin_convertvector(v, bf16x2)); }
DI float bflo(unsigned u) { return __uint_as_float(u << 16); }
DI float bfhi(unsigned u) { return __uint_as_float(u & 0xffff0000u); }
DI f32x16 mf32(bf16x8 a, bf16x8 b, f32x16 c) { return __builtin_amdgcn_mfma_f32_32x32x16_bf16(a, b, c, 0, 0, 0); }
DI f32x4 mf16(bf16x8 a, bf16x8 b, f32x4 c) { return __builtin_amdgcn_mfma_f32_16x16x32_bf16(a, b, c, 0, 0, 0); }
DI float uphalf(float v) {
    auto r = __builtin_amdgcn_permlane32_swap(__float_as_uint(v), __float_as_uint(v), false, false); return __uint_as_float(r[1]); }
DI float sigm(float v) { return __builtin_amdgcn_rcpf(1.f + __builtin_amdgcn_exp2f(-1.4426950408889634f * v)); }
template <int SH, int SLP = 1> DI u64 pollw(const char* p, u64 want) {
    u64 v;
    for (unsigned n = 0;; ++n) {
        v = __hip_atomic_load((const GAS u64*)p, RLX);
        if (__all((v >> SH) == want) || n > SPIN_CAP) break;
        __builtin_amdgcn_s_sleep(SLP);
    }
    asm volatile("" ::: "memory");
    return v;
}
DI void stflag(char* p, u64 v) { __hip_atomic_store((GAS u64*)p, v, RLX); }
DI unsigned top8(unsigned a, unsigned b, int l) {
    unsigned res = 0;
#pragma unroll 1
    for (int e = 0; e < 8; ++e) {
        unsigned m = a > b ? a : b;
        m = max(m, (unsigned)__builtin_amdgcn_update_dpp(0, (int)m, 0x111, 0xf, 0xf, true));
        m = max(m, (unsigned)__builtin_amdgcn_update_dpp(0, (int)m, 0x112, 0xf, 0xf, true));
        m = max(m, (unsigned)__builtin_amdgcn_update_dpp(0, (int)m, 0x114, 0xf, 0xf, true));
        m = max(m, (unsigned)__builtin_amdgcn_update_dpp(0, (int)m, 0x118, 0xf, 0xf, true));
        m = max(m, (unsigned)__builtin_amdgcn_update_dpp(0, (int)m, 0x142, 0xa, 0xf, true));
        m = max(m, (unsigned)__builtin_amdgcn_update_dpp(0, (int)m, 0x143, 0xc, 0xf, true));
        const unsigned w = (unsigned)__builtin_amdgcn_readlane((int)m, 63);
        a = a == w ? 0u : a; b = b == w ? 0u : b;
        res = l == e ? w : res;
    }
    return res;
}
DI unsigned hsync(unsigned addr, bool wait, int l) {
    unsigned old = 0;
    asm volatile("s_waitcnt lgkmcnt(0)" ::: "memory");
    if (l == 0) old = __hip_atomic_fetch_add((LAS unsigned*)addr, 1u, __ATOMIC_RELAXED, __HIP_MEMORY_SCOPE_WORKGROUP);
    old = __builtin_amdgcn_readfirstlane(old);
    if (wait)
        for (unsigned n = 0; n < (SPIN_CAP << 3); ++n) {
            if (__builtin_amdgcn_readfirstlane(__hip_atomic_load((LAS unsigned*)addr, __ATOMIC_RELAXED, __HIP_MEMORY_SCOPE_WORKGROUP)) >= 4u) break;
            __builtin_amdgcn_s_sleep(2);
        }
    asm volatile("" ::: "memory");
    return old;
}
#define STAMP(k)
#define STAMPC(k)
#define DUMP()

DI void epilogue(char* out, int wv, int l, int t, int idx, u32x4 xres) {
    const int tok = 4 * wv + (l >> 4), ch = l & 15, ct = idx & 3;
    const u32x4 a = ldsr<u32x4>(L_T + tok * 272 + ch * 16);
    const float g = ldsr<float>(L_GS + tok * 4), g1 = 1.f - g;
    const int e = ldsr<int>(L_MAP + tok * 4);
    f32x4 y0 = {0.f, 0.f, 0.f, 0.f}, y1 = y0;
    if (e >= 0) { y0 = ldsr<f32x4>(L_YS + e * 512 + ch * 32); y1 = ldsr<f32x4>(L_YS + e * 512 + ch * 32 + 16); }
    u32x4 o;
    o[0] = pk(g * bflo(a[0]) + g1 * (bflo(xres[0]) + y0[0]), g * bfhi(a[0]) + g1 * (bfhi(xres[0]) + y0[1]));
    o[1] = pk(g * bflo(a[1]) + g1 * (bflo(xres[1]) + y0[2]), g * bfhi(a[1]) + g1 * (bfhi(xres[1]) + y0[3]));
    o[2] = pk(g * bflo(a[2]) + g1 * (bflo(xres[2]) + y1[0]), g * bfhi(a[2]) + g1 * (bfhi(xres[2]) + y1[1]));
    o[3] = pk(g * bflo(a[3]) + g1 * (bflo(xres[3]) + y1[2]), g * bfhi(a[3]) + g1 * (bfhi(xres[3]) + y1[3]));
    if (ct < 3 || ch < 8) *(u32x4*)(out + (size_t)(t * 128 + (idx >> 2) * 32 + tok) * 896 + ct * 256 + ch * 16) = o;
}
DI u32x4 load_res(const char* x, int wv, int l, int t, int idx) {
    const int tok = 4 * wv + (l >> 4), ch = l & 15, ct = idx & 3;
    const int c = (ct < 3 || ch < 8) ? ch : 0;
    return *(const u32x4*)(x + (size_t)(t * 128 + (idx >> 2) * 32 + tok) * 896 + ct * 256 + c * 16);
}

__global__ __launch_bounds__(512, 2) void fused_kernel(const char* __restrict__ x, const char* __restrict__ wqkv, const char* __restrict__ wo,
                                                       const char* __restrict__ wr, const char* __restrict__ wgate, const char* __restrict__ wup,
                                                       const char* __restrict__ wdown, const char* __restrict__ gw, char* __restrict__ out, char* ws) {
    const int b = blockIdx.x, xcd = b & 7, i = b >> 3;
    const int t = (xcd & 3) * 4 + (i & 3), idx = (i >> 2) * 2 + (xcd >> 2);
    const int wv = __builtin_amdgcn_readfirstlane(threadIdx.x >> 6), l = threadIdx.x & 63;
    const int h2 = l >> 5, r = l & 31, r8 = l >> 3, c8 = l & 7;
    const int tq = (l & 15) >> 2, tp = l & 3, blk = (l >> 4) & 1;
    const bool spare = idx >= 14;
    STAMP(0);
    if (wv < 4) {
        const rsrc_t rx = mkrs(x, 2048 * 896);
        const unsigned xrow = (t * 128 + wv * 32 + r8) * 896;
        const unsigned xa = xrow + ((c8 ^ (r8 >> 1)) << 4), xb = xrow + ((c8 ^ (r8 >> 1) ^ 4) << 4);
        const unsigned xv0 = xa, xv1 = xb + 6144, xv2 = xa + 2 * 6144, xv3 = xb + 3 * 6144;
        const int rot = (i >> 2) == 7 ? 0 : (i >> 2);
        const unsigned xr = L_XR + wv * 16384;
#define CH(p) ((p) + rot >= 7 ? (p) + rot - 7 : (p) + rot)
#pragma unroll
        for (int p = 0; p < 4; ++p) dma4(xr + p * 4096, xv0, xv1, xv2, xv3, rx, CH(p) * 128);
        STAMP(1);
        const rsrc_t rkv = mkrs(ws + WS_KV, 4u << 20);
        const bool isK = idx < 7;
        unsigned so[8];
#pragma unroll
        for (int q = 0; q < 8; ++q) { const unsigned G = 16 * idx + 2 * q + h2, hh = (G * 147) >> 10; so[q] = hh * 262144 + (G - 7 * hh) * 256 + r * 8; }
        if (!isK) {
#pragma unroll
            for (int n = 0; n < 2; ++n) { const unsigned C = 64 * (idx - 7) + 32 * n + r, hh = (C * 2341) >> 16; so[n] = hh * 262144 + 2048 + h2 * 512 + (C - 28 * hh) * 16; }
        }
        unsigned xad[4];
#pragma unroll
        for (int s = 0; s < 4; ++s) xad[s] = xr + r * 128 + (((((r >> 1) & 7) ^ h2) << 4) ^ (32 * s));
        const unsigned wad = L_WKV + (8 * h2 + tq) * 128 + (((blk * 2 + (tp >> 1)) ^ ((tq >> 1) << 2)) << 4) + (tp & 1) * 8;
        bf16x8 xf[28];
        f32x16 acc0 = {}, acc1 = {};
        WAITV(12);
        BAR();
        STAMP(2);
        const u64 tag = ldsr<u64>(L_CTL);
#define RING(KIND) _Pragma("unroll") for (int p = 0; p < 7; ++p) { \
            if (p == 1 || p == 2 || p == 3) WAITV(12); else if (p == 4) WAITV(8); else if (p == 5) WAITV(4); else if (p == 6) WAITV(0); \
            _Pragma("unroll") for (int s = 0; s < 4; ++s) xf[4 * p + s] = ldsr<bf16x8>(xad[s] + (p & 3) * 4096); \
            const unsigned w0 = wad + CH(p) * 8192, w1 = w0 ^ 64; \
            _Pragma("unroll") for (int s = 0; s < 4; ++s) { \
                if (KIND == 0) { acc0 = mf32(tr8(w0 + s * 2048, w0 + s * 2048 + 512), xf[4 * p + s], acc0); acc1 = mf32(tr8(w1 + s * 2048, w1 + s * 2048 + 512), xf[4 * p + s], acc1); } \
                if (KIND == 1) { acc0 = mf32(xf[4 * p + s], tr8(w0 + s * 2048, w0 + s * 2048 + 512), acc0); acc1 = mf32(xf[4 * p + s], tr8(w1 + s * 2048, w1 + s * 2048 + 512), acc1); } \
            } \
            if (p < 3) { asm volatile("s_waitcnt lgkmcnt(0)" ::: "memory"); dma4(xr + p * 4096, xv0, xv1, xv2, xv3, rx, CH(p + 4) * 128); } \
        }
        if (isK) { RING(0) } else if (!spare) { RING(1) } else { RING(2) }
        STAMP(3);
        const unsigned tso = (t * 4 + wv) * 4096;
        if (isK) {
#pragma unroll
            for (int q = 0; q < 4; ++q) {
                __builtin_amdgcn_raw_buffer_store_b64((u32x2){pk(acc0[4 * q], acc0[4 * q + 1]), pk(acc0[4 * q + 2], acc0[4 * q + 3])}, rkv, so[q], tso, 16);
                __builtin_amdgcn_raw_buffer_store_b64((u32x2){pk(acc1[4 * q], acc1[4 * q + 1]), pk(acc1[4 * q + 2], acc1[4 * q + 3])}, rkv, so[4 + q], tso, 16);
            }
        } else if (!spare) {
#pragma unroll
            for (int s = 0; s < 2; ++s) {
                __builtin_amdgcn_raw_buffer_store_b128((u32x4){pk(acc0[8 * s], acc0[8 * s + 1]), pk(acc0[8 * s + 2], acc0[8 * s + 3]), pk(acc0[8 * s + 4], acc0[8 * s + 5]), pk(acc0[8 * s + 6], acc0[8 * s + 7])}, rkv, so[0] + s * 1024, tso, 16);
                __builtin_amdgcn_raw_buffer_store_b128((u32x4){pk(acc1[8 * s], acc1[8 * s + 1]), pk(acc1[8 * s + 2], acc1[8 * s + 3]), pk(acc1[8 * s + 4], acc1[8 * s + 5]), pk(acc1[8 * s + 6], acc1[8 * s + 7])}, rkv, so[1] + s * 1024, tso, 16);
            }
        }
        BAR();
        WAITV(0);
        BAR();
        if (!spare && wv == 0 && l == 0) stflag(ws + WS_KVFLAG + (t * 16 + idx) * 128, tag);
        STAMP(4);
        BAR();
        const unsigned qad = L_WQ + (8 * h2 + tq) * 64 + ((((idx * 56) & 15) + blk * 32 + tp * 8) & 63);
        f32x16 aq = {};
#pragma unroll
        for (int p = 0; p < 7; ++p) {
            const unsigned q0 = qad + CH(p) * 4096;
#pragma unroll
            for (int s = 0; s < 4; ++s) aq = mf32(tr8(q0 + s * 1024, q0 + s * 1024 + 256), xf[4 * p + s], aq);
        }
        const float gate = sigm(aq[13]);
        if (idx == 14 && h2) {
            const unsigned u = pk(aq[12], 0.f) & 0xffffu;
            const unsigned o = (u & 0x7fffu) > 0x7f80u ? 0u : (u ^ ((u & 0x8000u) ? 0xffffu : 0x8000u));
            ldsw<unsigned>(L_KEY + (wv * 32 + r) * 4, (o << 11) | (2047 - (t * 128 + wv * 32 + r)));
        }
        constexpr float QS = 0.18898223650461361f * 1.4426950408889634f;
        {
            u32x4 q0 = {pk(aq[0] * QS, aq[1] * QS), pk(aq[2] * QS, aq[3] * QS), pk(aq[4] * QS, aq[5] * QS), pk(aq[6] * QS, aq[7] * QS)};
            u32x4 q1 = {pk(aq[8] * QS, aq[9] * QS), pk(aq[10] * QS, aq[11] * QS), pk(aq[12] * QS, aq[13] * QS), pk(aq[14] * QS, aq[15] * QS)};
            ldsw<u32x4>(L_QX + wv * 2048 + l * 16, q0);
            ldsw<u32x4>(L_QX + wv * 2048 + 1024 + l * 16, q1);
        }
        BAR();
        STAMP(5);
        u32x4 q00, q01, q10, q11, q20, q21, q30, q31;
        q00 = ldsr<u32x4>(L_QX + wv * 2048 + l * 16);             q01 = ldsr<u32x4>(L_QX + wv * 2048 + 1024 + l * 16);
        q10 = ldsr<u32x4>(L_QX + ((wv + 1) & 3) * 2048 + l * 16); q11 = ldsr<u32x4>(L_QX + ((wv + 1) & 3) * 2048 + 1024 + l * 16);
        q20 = ldsr<u32x4>(L_QX + ((wv + 2) & 3) * 2048 + l * 16); q21 = ldsr<u32x4>(L_QX + ((wv + 2) & 3) * 2048 + 1024 + l * 16);
        q30 = ldsr<u32x4>(L_QX + ((wv + 3) & 3) * 2048 + l * 16); q31 = ldsr<u32x4>(L_QX + ((wv + 3) & 3) * 2048 + 1024 + l * 16);
        const u32x4 xres = load_res(x, wv, l, t, idx);
        {
            const int js = l & 3, j = (((7 * idx + ((js & 1) ? 6 : 0)) >> 4) + ((js & 2) ? 7 : 0));
            pollw<0>(ws + WS_KVFLAG + ((4 * wv + ((l >> 2) & 3)) * 16 + j) * 128, tag);
        }
        STAMP(6); STAMPC(13);
        const rsrc_t ratt = mkrs(ws + WS_KV + idx * 262144 + wv * 65536, 65536);
        f32x16 o0, o1, o2, o3; float m0, m1, m2, m3;
#pragma unroll 1
        for (unsigned mode = 0;; ++mode) {
            asm volatile("" : "+s"(mode));
            unsigned s_so, s_sk, s_s2, s_cn;
            asm volatile(ATTN_ASM
                : "=&{v[64:79]}"(o0), "=&{v[80:95]}"(o1), "=&{v[96:111]}"(o2), "=&{v[112:127]}"(o3),
                  "=&{v240}"(m0), "=&{v241}"(m1), "=&{v242}"(m2), "=&{v243}"(m3),
                  "+{v[36:39]}"(q01), "+{v[44:47]}"(q11), "+{v[52:55]}"(q21), "+{v[60:63]}"(q31),
                  [so] "=&s"(s_so), [sk] "=&s"(s_sk), [s2] "=&s"(s_s2), [cn] "=&s"(s_cn)
                : "{v[32:35]}"(q00), "{v[40:43]}"(q10), "{v[48:51]}"(q20), "{v[56:59]}"(q30),
                  [vk] "v"(l * 8), [vv] "v"(l * 16), [rs] "s"(ratt), [st] "s"((t & 3) * 16384), [md] "s"(mode)
                : ATTN_CLOB, "scc", "memory");
            const float lo = 8.6736174e-19f, hi = 1.1529215e18f;
            const bool ok = o0[12] > lo && o0[12] < hi && o1[12] > lo && o1[12] < hi && o2[12] > lo && o2[12] < hi && o3[12] > lo && o3[12] < hi;
            if (mode || (__ballot(!ok) >> 32) == 0) break;
        }
        STAMP(7); STAMPC(14);
        int l_ = l; asm volatile("" : "+v"(l_));

        {
        const int l = l_, h2 = l >> 5, r = l & 31, tq = (l & 15) >> 2, tp = l & 3, blk = (l >> 4) & 1;
        BAR();
        if (h2) { o1[13] = m1; o2[13] = m2; o3[13] = m3; }
#pragma unroll
        for (int q = 0; q < 4; ++q) {
            ldsw<f32x4>(L_MS + (((wv + 1) & 3) * 3 + 0) * 4096 + q * 1024 + l * 16, (f32x4){o1[4 * q], o1[4 * q + 1], o1[4 * q + 2], o1[4 * q + 3]});
            ldsw<f32x4>(L_MS + (((wv + 2) & 3) * 3 + 1) * 4096 + q * 1024 + l * 16, (f32x4){o2[4 * q], o2[4 * q + 1], o2[4 * q + 2], o2[4 * q + 3]});
            ldsw<f32x4>(L_MS + (((wv + 3) & 3) * 3 + 2) * 4096 + q * 1024 + l * 16, (f32x4){o3[4 * q], o3[4 * q + 1], o3[4 * q + 2], o3[4 * q + 3]});
        }
        BAR();
        {
            f32x4 pa[4], pb[4], pc[4];
#pragma unroll
            for (int q = 0; q < 4; ++q) {
                pa[q] = ldsr<f32x4>(L_MS + (wv * 3 + 0) * 4096 + q * 1024 + l * 16);
                pb[q] = ldsr<f32x4>(L_MS + (wv * 3 + 1) * 4096 + q * 1024 + l * 16);
                pc[q] = ldsr<f32x4>(L_MS + (wv * 3 + 2) * 4096 + q * 1024 + l * 16);
            }
            const float ma = uphalf(pa[3][1]), mb = uphalf(pb[3][1]), mc = uphalf(pc[3][1]);
            const float mm = fmaxf(fmaxf(m0, ma), fmaxf(mb, mc));
            float f0 = __builtin_amdgcn_exp2f(m0 - mm), fa = __builtin_amdgcn_exp2f(ma - mm), fb = __builtin_amdgcn_exp2f(mb - mm), fc = __builtin_amdgcn_exp2f(mc - mm);
            const float inv = __builtin_amdgcn_rcpf(uphalf(f0 * o0[12] + fa * pa[3][0] + fb * pb[3][0] + fc * pc[3][0]));
            f0 *= inv; fa *= inv; fb *= inv; fc *= inv;
            float v[16];
#pragma unroll
            for (int k = 0; k < 16; ++k) v[k] = f0 * o0[k] + fa * pa[k >> 2][k & 3] + fb * pb[k >> 2][k & 3] + fc * pc[k >> 2][k & 3];
            const rsrc_t ro = mkrs(ws + WS_O, 2u << 20);
            const unsigned oso = ((t * 4 + wv) * 16 + idx) * 2048;
            __builtin_amdgcn_raw_buffer_store_b128((u32x4){pk(v[0], v[1]), pk(v[2], v[3]), pk(v[4], v[5]), pk(v[6], v[7])}, ro, l * 16, oso, 16);
            __builtin_amdgcn_raw_buffer_store_b128((u32x4){pk(v[8], v[9]), pk(v[10], v[11]), h2 ? 0u : pk(v[12], v[13]), h2 ? 0u : pk(v[14], v[15])}, ro, l * 16 + 1024, oso, 16);
        }
        STAMP(8);
        const bool act = (idx & 3) < 3 || wv < 2;
        bf16x8 wf[32];
        {
            const unsigned a0 = L_WO + (4 * h2 + tq) * 256 + (((wv * 4 + blk * 2 + (tp >> 1)) ^ (tq << 2)) << 4) + (tp & 1) * 8;
            const unsigned a1 = a0 + 8 * 28 * 256, a2 = a1 - h2 * 1024;
#pragma unroll
            for (int k = 0; k < 32; ++k) {
                const int hh = k >> 1, s = k & 1;
                const unsigned base = hh < 8 ? a0 : a1, off = (28 * (hh & 7) + 16 * s) * 256;
                wf[k] = tr8(base + off, (k == 31 ? a2 : base) + off + 2048);
            }
        }
        WAITV(0);
        BAR();
        if (wv == 0 && l == 0) stflag(ws + WS_OFLAG + (t * 16 + idx) * 128, tag);
        STAMP(9);
        if (wv == (idx >> 2) && h2) ldsw<float>(L_GS + r * 4, gate);
        BAR();
        STAMP(10);
        f32x16 ac = {};
        if (act) {
#pragma unroll
            for (int k = 0; k < 32; ++k) ac = mf32(wf[k], ldsr<bf16x8>(L_OF + k * 1024 + l * 16), ac);
#pragma unroll
            for (int q = 0; q < 4; ++q) ldsw<u32x2>(L_T + r * 272 + (32 * wv + 8 * q + 4 * h2) * 2, (u32x2){pk(ac[4 * q], ac[4 * q + 1]), pk(ac[4 * q + 2], ac[4 * q + 3])});
        }
        BAR();
        STAMP(11);
        epilogue(out, wv, l, t, idx, xres);
        STAMP(12); DUMP();
        }
    } else {
        const int hw = wv - 4;
        __builtin_amdgcn_s_setprio(3);
        const rsrc_t rw = mkrs(wqkv, 448 * 2688);
        const unsigned wqv = (l >> 2) * 2688 + ((idx * 56) & ~15) + (l & 3) * 16;
        if (!spare) {
            const unsigned v0 = r8 * 2688 + 896 + idx * 128 + ((c8 ^ (((r8 >> 1) & 1) << 2)) << 4), v1 = v0 + 21504 - 1024;
            int g = (t & 3) * 2;
#pragma unroll 1
            for (int n = 0; n < 7; ++n) { const int p = 14 * hw + 2 * g; dma2(L_WKV + p * 1024, v0, v1, rw, p * 21504); g = g == 6 ? 0 : g + 1; }
        } else {
#pragma unroll 1
            for (int n = 0; n < 7; ++n) { const int p = 7 * hw + n; dma1(L_WQ + p * 1024, wqv, rw, p * 43008); }
        }
        STAMP(1);
        const u64 tag = __hip_atomic_load((const GAS u64*)(ws + WS_EPOCH), RLX) + 1;
        const int lc = l < 56 ? l : 55;
        const unsigned wr2 = *(const unsigned*)(wr + (112 * hw + 2 * lc) * 2), gw2 = *(const unsigned*)(gw + (112 * hw + 2 * lc) * 2);
        if (!spare) {
            const int jj = idx < 7 ? idx : idx - 7, hh = (16 * jj) / 7 + (l >> 4), li = l & 15;
            const rsrc_t rkv = mkrs(ws + WS_KV, 4u << 20);
            const unsigned one = (idx < 7 || (li & 3) == 0) ? 0x3f803f80u : 0u, w1 = idx < 7 ? 0x00003f80u : one;
            const unsigned off = idx < 7 ? 7 * 256 + li * 16 : 2048 + (li >> 3) * 1024 + ((li >> 2) & 1) * 512 + (28 + (li & 3)) * 16;
            if (hh < (16 * (jj + 1)) / 7) __builtin_amdgcn_raw_buffer_store_b128((u32x4){one, w1, one, w1}, rkv, hh * 262144 + off, (t * 4 + hw) * 4096, 16);
        }
        if (hw == 0 && l == 0) { ldsw<u64>(L_CTL, tag); ldsw<u32x4>(L_CTL + 16, (u32x4){0u, 0u, 0u, 0u}); }
        const unsigned pad = L_WQ + (112 * hw + 2 * lc) * 64 + ((((idx * 56) & 15) + 56) & 63);
        const u32x2 pad0 = {(wr2 & 0xffffu) | (gw2 << 16), 0u}, pad1 = {(wr2 >> 16) | (gw2 & 0xffff0000u), 0u};
        WAITV(0);
        if (spare) { ldsw<u32x2>(pad, pad0); ldsw<u32x2>(pad + 64, pad1); }
        BAR();
        STAMP(2);
        const int mid = idx == 15 ? t : 16 + t;
        const bool moe = spare && mid < 28;
        const rsrc_t rg = mkrs((hw >> 1) ? wup : wgate, 448 * 3584);
        const unsigned gph = (r8 >> 1) & 1;
        const unsigned gv0 = (224 * (hw & 1) + r8) * 3584 + mid * 128 + ((c8 ^ (gph << 1)) << 4);
        const unsigned gv1 = (224 * (hw & 1) + r8 + 8) * 3584 + mid * 128 + ((c8 ^ ((gph | 2) << 1)) << 4) - 1024;
        const unsigned areaA = hw * 14336, areaB = L_XR + hw * 16384;
        if (moe) {
#pragma unroll 1
            for (int n = 0; n < 6; ++n) dma2(areaA + n * 2048, gv0, gv1, rg, n * 16 * 3584);
        }
        BAR();
        if (!spare) {
#pragma unroll 1
            for (int n = 0; n < 7; ++n) { const int p = 7 * hw + n; dma1(L_WQ + p * 1024, wqv, rw, p * 43008); }
        } else if (moe) {
#pragma unroll 1
            for (int n = 0; n < 8; ++n) dma2(areaB + n * 2048, gv0, gv1, rg, (n + 6) * 16 * 3584);
        }
        BAR();
        if (!spare) {
            WAITV(0);
            ldsw<u32x2>(pad, pad0); ldsw<u32x2>(pad + 64, pad1);
        }
        STAMP(4);
        BAR();
        BAR();
        STAMP(5);
        if (idx == 14 && hw == 0) {
            const unsigned k = top8(ldsr<unsigned>(L_KEY + l * 4), ldsr<unsigned>(L_KEY + 256 + l * 4), l);
            if (l < 8) stflag(ws + WS_CAND + t * 128 + l * 8, (tag << 27) | k);
            if (t == 15) {
                const char* cp = ws + WS_CAND + (l >> 3) * 128 + (l & 7) * 8;
                const unsigned ka = (unsigned)pollw<27>(cp, tag & TAG37) & 0x7ffffffu, kb = (unsigned)pollw<27>(cp + 1024, tag & TAG37) & 0x7ffffffu;
                const unsigned s = top8(ka, kb, l);
                if (l < 8) stflag(ws + WS_SEL + l * 8, (tag << 27) | s);
            }
        }
        const u32x4 xres = load_res(x, wv, l, t, idx);
        const rsrc_t rwo = mkrs(wo, 448 * 896);
        const unsigned wov0 = (l >> 4) * 896 + (idx & 3) * 256 + (((l & 15) ^ ((l >> 4) << 2)) << 4), wov1 = wov0 + 3584 - 1024;
        unsigned key;
        if (moe) {
            bf16x8 gf[28], df[14];
            const int g4 = l >> 4;
            const unsigned gad = (8 * g4 + tq) * 128 + (tp >> 1) * 16 + (tp & 1) * 8, gx = ((tq >> 1) | ((g4 & 1) << 1)) << 5;
            WAITV(16);
#pragma unroll
            for (int ks = 0; ks < 3; ++ks)
#pragma unroll
                for (int c = 0; c < 4; ++c) { const unsigned a = areaA + gad + ((c << 5) ^ gx) + ks * 4096; gf[ks * 4 + c] = tr8(a, a + 512); }
            WAITV(0);
#pragma unroll
            for (int ks = 3; ks < 7; ++ks)
#pragma unroll
                for (int c = 0; c < 4; ++c) { const unsigned a = areaB + gad + ((c << 5) ^ gx) + (ks - 3) * 4096; gf[ks * 4 + c] = tr8(a, a + 512); }
            asm volatile("s_waitcnt lgkmcnt(0)" ::: "memory");
            const rsrc_t rd = mkrs(wdown, 1792 * 896);
            const unsigned dbase = (64 * mid + g4) * 896 + hw * 224;
            const unsigned dv0 = dbase + (((l & 15) ^ (g4 << 1)) << 4), dv1 = dv0 + 3584 - 1024;
            const unsigned dv2 = dbase + (((l & 15) ^ ((g4 | 4) << 1)) << 4) + 2 * (3584 - 1024), dv3 = dv2 + 3584 - 1024;
#pragma unroll 1
            for (int n = 0; n < 4; ++n) dma4(areaB + n * 4096, dv0, dv1, dv2, dv3, rd, n * 16 * 896);
            WAITV(0);
            const unsigned dad = areaB + (8 * g4 + tq) * 256 + (tp >> 1) * 16 + (tp & 1) * 8, dx = (tq | ((g4 & 1) << 2)) << 5;
#pragma unroll
            for (int ks = 0; ks < 2; ++ks)
#pragma unroll
                for (int c = 0; c < 7; ++c) { const unsigned a = dad + ((c << 5) ^ dx) + ks * 8192; df[ks * 7 + c] = tr8(a, a + 1024); }
            STAMP(6);
            key = (unsigned)pollw<27, 16>(ws + WS_SEL + (l & 7) * 8, tag & TAG37) & 0x7ffffffu;
            const char* xs = x + (size_t)(2047 - (key & 2047u)) * 896 + (224 * (hw & 1) + 8 * g4) * 2;
            f32x4 pa[4] = {};
#pragma unroll
            for (int ks = 0; ks < 7; ++ks) {
                const bf16x8 xb = *(const bf16x8*)(xs + ks * 64);
#pragma unroll
                for (int c = 0; c < 4; ++c) pa[c] = mf16(gf[ks * 4 + c], xb, pa[c]);
            }
#pragma unroll
            for (int c = 0; c < 4; ++c) ldsw<f32x4>(L_EX1 + hw * 4096 + c * 1024 + l * 16, pa[c]);
            hsync(L_CTL + 16, true, l);
            const f32x4 ga = ldsr<f32x4>(L_EX1 + hw * 1024 + l * 16) + ldsr<f32x4>(L_EX1 + 4096 + hw * 1024 + l * 16);
            const f32x4 ua = ldsr<f32x4>(L_EX1 + 8192 + hw * 1024 + l * 16) + ldsr<f32x4>(L_EX1 + 12288 + hw * 1024 + l * 16);
            ldsw<u32x2>(L_EX2 + (l & 15) * 144 + (16 * hw + 4 * g4) * 2,
                        (u32x2){pk(ga[0] * sigm(ga[0]) * ua[0], ga[1] * sigm(ga[1]) * ua[1]), pk(ga[2] * sigm(ga[2]) * ua[2], ga[3] * sigm(ga[3]) * ua[3])});
            hsync(L_CTL + 20, true, l);
            const bf16x8 h0 = ldsr<bf16x8>(L_EX2 + (l & 15) * 144 + 16 * g4), h1 = ldsr<bf16x8>(L_EX2 + (l & 15) * 144 + 64 + 16 * g4);
            const rsrc_t ry = mkrs(ws + WS_YP, 28 * 14336);
#pragma unroll
            for (int c = 0; c < 7; ++c) {
                const f32x4 y = mf16(df[7 + c], h1, mf16(df[c], h0, (f32x4){0.f, 0.f, 0.f, 0.f}));
                if ((l & 15) < 8) __builtin_amdgcn_raw_buffer_store_b128(__builtin_bit_cast(u32x4, y), ry, (l & 15) * 1792 + (112 * hw + 16 * c + 4 * g4) * 4, mid * 14336, 16);
            }
            WAITV(0);
            if (hsync(L_CTL + 24, false, l) == 3 && l == 0) stflag(ws + WS_MFLAG + mid * 128, tag);
            STAMP(7);
        } else {
            __builtin_amdgcn_s_sleep(40);
        }
#pragma unroll 1
        for (int n = 0; n < 14; ++n) { const int p = 28 * hw + 2 * n; dma2(L_WO + p * 1024, wov0, wov1, rwo, p * 3584); WAITV(4); }
        if (!moe) key = (unsigned)pollw<27, 16>(ws + WS_SEL + (l & 7) * 8, tag & TAG37) & 0x7ffffffu;
        const int tl = (2047 - (int)(key & 2047u)) - (t * 128 + (idx >> 2) * 32);
        const unsigned hit = (unsigned)__ballot(tl >= 0 && tl < 32) & 0xffu;
        f32x4 ya[2], yb[2];
        if (((hit >> hw) & 0x11u) || (b == 0 && hw == 0)) pollw<0, 16>(ws + WS_MFLAG + (l < 28 ? l : 0) * 128, tag);
#pragma unroll
        for (int u = 0; u < 2; ++u) {
            const int e = hw + 4 * u;
            ya[u] = (f32x4){0.f, 0.f, 0.f, 0.f}; yb[u] = ya[u];
            if ((hit >> e) & 1) {
                const rsrc_t ry = mkrs(ws + WS_YP, 28 * 14336);
                const int cc = ((idx & 3) < 3 || (l & 15) < 8) ? (l & 15) : 0;
                const unsigned yo = (l >> 4) * 14336 + e * 1792 + ((idx & 3) * 128 + cc * 8) * 4;
#pragma unroll
                for (int p = 0; p < 7; ++p) {
                    ya[u] += __builtin_bit_cast(f32x4, __builtin_amdgcn_raw_buffer_load_b128(ry, yo, p * 4 * 14336, 16));
                    yb[u] += __builtin_bit_cast(f32x4, __builtin_amdgcn_raw_buffer_load_b128(ry, yo + 16, p * 4 * 14336, 16));
                }
                const unsigned ko = (unsigned)__builtin_amdgcn_readlane((int)key, e) >> 11;
                const float rs = sigm(bflo(ko ^ ((ko & 0x8000u) ? 0x8000u : 0xffffu)));
#pragma unroll
                for (int k = 0; k < 4; ++k) {
                    float a = ya[u][k], c = yb[u][k];
                    a += __shfl_xor(a, 16); a += __shfl_xor(a, 32); c += __shfl_xor(c, 16); c += __shfl_xor(c, 32);
                    ya[u][k] = a * rs; yb[u][k] = c * rs;
                }
            }
        }
        WAITV(0);
        STAMP(8);
        BAR();
        BAR();
        BAR();
        STAMP(9);
        if (hw == 1 && l < 32) {
            int e = -1;
#pragma unroll
            for (int k = 0; k < 8; ++k) e = __builtin_amdgcn_readlane(tl, k) == l ? k : e;
            ldsw<int>(L_MAP + l * 4, e);
        }
#pragma unroll
        for (int u = 0; u < 2; ++u)
            if (((hit >> (hw + 4 * u)) & 1) && l < 16) { ldsw<f32x4>(L_YS + (hw + 4 * u) * 512 + l * 32, ya[u]); ldsw<f32x4>(L_YS + (hw + 4 * u) * 512 + l * 32 + 16, yb[u]); }
        pollw<0>(ws + WS_OFLAG + (t * 16 + 4 * hw + (l & 3)) * 128, tag);
        STAMP(10);
        {
            const rsrc_t ro = mkrs(ws + WS_O, 2u << 20);
            const unsigned oso = ((t * 4 + (idx >> 2)) * 16 + 4 * hw) * 2048;
#pragma unroll
            for (int n = 0; n < 8; ++n) dma1c(L_OF + (8 * hw + n) * 1024, l * 16, ro, oso + n * 1024);
        }
        WAITV(0);
        BAR();
        if (b == 0 && hw == 0 && l == 0) stflag(ws + WS_EPOCH, tag);
        BAR();
        STAMP(11);
        epilogue(out, wv, l, t, idx, xres);
        STAMP(12); DUMP();
    }
}

extern "C" void kernel_launch(void* const* d_in, const int* in_sizes, int n_in, void* d_out, int out_size, void* d_ws, size_t ws_size,
                              hipStream_t stream) {
    (void)in_sizes; (void)n_in; (void)out_size; (void)ws_size;
    (void)hipFuncSetAttribute((const void*)fused_kernel, hipFuncAttributeMaxDynamicSharedMemorySize, LDS_BYTES);
    fused_kernel<<<256, 512, LDS_BYTES, stream>>>((const char*)d_in[0], (const char*)d_in[1], (const char*)d_in[2], (const char*)d_in[4],
                                                  (const char*)d_in[5], (const char*)d_in[6], (const char*)d_in[7], (const char*)d_in[8],
                                                  (char*)d_out, (char*)d_ws);
}
```

```cpp
#include <hip/hip_runtime.h>
#include <hip/hip_bf16.h>

#define LAS __attribute__((address_space(3)))
#define GAS __attribute__((address_space(1)))
#define DI __device__ __forceinline__
typedef float f32x16 __attribute__((ext_vector_type(16)));
typedef float f32x4 __attribute__((ext_vector_type(4)));
typedef float f32x2 __attribute__((ext_vector_type(2)));
typedef __bf16 bf16x8 __attribute__((ext_vector_type(8)));
typedef __bf16 bf16x2 __attribute__((ext_vector_type(2)));
typedef short s16x4 __attribute__((ext_vector_type(4)));
typedef unsigned u32x4 __attribute__((ext_vector_type(4)));
typedef unsigned u32x2 __attribute__((ext_vector_type(2)));
typedef unsigned u32x8 __attribute__((ext_vector_type(8)));
typedef unsigned u32x16 __attribute__((ext_vector_type(16)));
typedef unsigned long long u64;
typedef __amdgpu_buffer_rsrc_t rsrc_t;

constexpr unsigned WS_EPOCH = 0, WS_KVFLAG = 4096, WS_OFLAG = 36864, WS_CAND = 69632, WS_SEL = 71680, WS_MFLAG = 73728,
                   WS_YP = 81920, WS_KV = 1u << 20, WS_O = 5u << 20, WS_DBG = 8u << 20;
constexpr unsigned L_WKV = 0, L_XR = 57344, L_WQ = 122880, L_KEY = 151552, L_QX = 155136, L_CTL = 163328;
constexpr unsigned L_WO = 0, L_MS = 114688, L_EX2 = 61440, L_ALT = 90112, L_XS = 147456;
constexpr unsigned L_OF = 114688, L_T = 147456, L_GS = 156672, L_MAP = 156928, L_YS = 157184, L_RDY = 163836;
constexpr unsigned LDS_BYTES = 163840;
constexpr unsigned SPIN_CAP = 1u << 17;
#define TX 4
#define T_SLEEP0 40
#define T_SLEEP1 12
#define T_SLEEP2 40
#define T_SLEEP3 8
#define T_TRIG 2
#define T_FLY 2
#define WAITX(n) WAITV(n)
static_assert(T_FLY <= 4 && T_FLY % 2 == 0, "CLEARED() relies on it");
constexpr unsigned EMPTY = 0xffffffffu;
constexpr u64 TAG37 = (1ull << 37) - 1;

#define MAIN_ASM \
    "v_lshlrev_b32 v1, 4, v0\n" \
    "v_lshlrev_b32 v2, 3, v0\n" \
    "s_mov_b32 s60, %[rot]\n" \
    "v_mov_b32 v3, 163328\n" \
    "s_mov_b32 s72, 114688\n" \
    "s_cmp_lt_u32 %[idx], 14\n" \
    "s_cselect_b32 s72, s72, 98304\n" \
    "v_add_u32 v9, s72, v1\n" \
    "s_add_u32 s61, %[wv], 1\n" \
    "s_and_b32 s61, s61, 3\n" \
    "s_mul_i32 s61, s61, 12288\n" \
    "s_add_u32 s61, s61, 0\n" \
    "v_add_u32 v28, s61, v9\n" \
    "s_add_u32 s61, %[wv], 2\n" \
    "s_and_b32 s61, s61, 3\n" \
    "s_mul_i32 s61, s61, 12288\n" \
    "s_add_u32 s61, s61, 4096\n" \
    "v_add_u32 v29, s61, v9\n" \
    "s_add_u32 s61, %[wv], 3\n" \
    "s_and_b32 s61, s61, 3\n" \
    "s_mul_i32 s61, s61, 12288\n" \
    "s_add_u32 s61, s61, 8192\n" \
    "v_add_u32 v30, s61, v9\n" \
    "s_mul_i32 s61, %[wv], 12288\n" \
    "v_add_u32 v31, s61, v9\n" \
    "v_mov_b32 v45, 0\n" \
    "ds_write_b32 v3, v45 offset:16\n" \
    "ds_write_b32 v3, v45 offset:20\n" \
    "ds_write_b32 v3, v45 offset:24\n" \
    "ds_write_b32 v3, v45 offset:28\n" \
    "s_waitcnt lgkmcnt(0)\n" \
    "s_cmp_lt_u32 %[idx], 7\n" \
    "s_cbranch_scc0 MN_NOTK_%=\n" \
    "s_lshl_b32 s61, s60, 13\n" \
    "v_add_u32 v20, s61, v24\n" \
    "v_xor_b32 v21, 64, v20\n" \
    "s_add_u32 s60, s60, 1\n" \
    "s_cmp_eq_u32 s60, 7\n" \
    "s_cselect_b32 s60, 0, s60\n" \
    "s_barrier\n" \
    "ds_read_b128 v[128:131], v16 offset:0\n" \
    "ds_read_b64_tr_b16 v[96:97], v20 offset:0\n" \
    "ds_read_b64_tr_b16 v[98:99], v20 offset:512\n" \
    "ds_read_b64_tr_b16 v[100:101], v21 offset:0\n" \
    "ds_read_b64_tr_b16 v[102:103], v21 offset:512\n" \
    "ds_read_b128 v[132:135], v17 offset:0\n" \
    "ds_read_b64_tr_b16 v[104:105], v20 offset:2048\n" \
    "ds_read_b64_tr_b16 v[106:107], v20 offset:2560\n" \
    "ds_read_b64_tr_b16 v[108:109], v21 offset:2048\n" \
    "ds_read_b64_tr_b16 v[110:111], v21 offset:2560\n" \
    "s_waitcnt lgkmcnt(7)\n" \
    "v_mfma_f32_32x32x16_bf16 v[64:79], v[96:99], v[128:131], 0\n" \
    "s_lshl_b32 s61, s60, 13\n" \
    "v_add_u32 v22, s61, v24\n" \
    "v_xor_b32 v23, 64, v22\n" \
    "s_add_u32 s60, s60, 1\n" \
    "s_cmp_eq_u32 s60, 7\n" \
    "s_cselect_b32 s60, 0, s60\n" \
    "ds_read_b128 v[136:139], v18 offset:0\n" \
    "ds_read_b64_tr_b16 v[112:113], v20 offset:4096\n" \
    "ds_read_b64_tr_b16 v[114:115], v20 offset:4608\n" \
    "s_waitcnt lgkmcnt(8)\n" \
    "v_mfma_f32_32x32x16_bf16 v[80:95], v[100:103], v[128:131], 0\n" \
    "ds_read_b64_tr_b16 v[116:117], v21 offset:4096\n" \
    "ds_read_b64_tr_b16 v[118:119], v21 offset:4608\n" \
    "s_waitcnt lgkmcnt(7)\n" \
    "v_mfma_f32_32x32x16_bf16 v[64:79], v[104:107], v[132:135], v[64:79]\n" \
    "ds_read_b128 v[140:143], v19 offset:0\n" \
    "ds_read_b64_tr_b16 v[120:121], v20 offset:6144\n" \
    "ds_read_b64_tr_b16 v[122:123], v20 offset:6656\n" \
    "s_waitcnt lgkmcnt(8)\n" \
    "v_mfma_f32_32x32x16_bf16 v[80:95], v[108:111], v[132:135], v[80:95]\n" \
    "ds_read_b64_tr_b16 v[124:125], v21 offset:6144\n" \
    "ds_read_b64_tr_b16 v[126:127], v21 offset:6656\n" \
    "s_waitcnt lgkmcnt(7)\n" \
    "v_mfma_f32_32x32x16_bf16 v[64:79], v[112:115], v[136:139], v[64:79]\n" \
    "s_waitcnt lgkmcnt(4)\n" \
    "s_barrier\n" \
    "ds_read_b128 v[144:147], v16 offset:4096\n" \
    "ds_read_b64_tr_b16 v[96:97], v22 offset:0\n" \
    "ds_read_b64_tr_b16 v[98:99], v22 offset:512\n" \
    "v_mfma_f32_32x32x16_bf16 v[80:95], v[116:119], v[136:139], v[80:95]\n" \
    "ds_read_b64_tr_b16 v[100:101], v23 offset:0\n" \
    "ds_read_b64_tr_b16 v[102:103], v23 offset:512\n" \
    "s_waitcnt lgkmcnt(7)\n" \
    "v_mfma_f32_32x32x16_bf16 v[64:79], v[120:123], v[140:143], v[64:79]\n" \
    "ds_read_b128 v[148:151], v17 offset:4096\n" \
    "ds_read_b64_tr_b16 v[104:105], v22 offset:2048\n" \
    "ds_read_b64_tr_b16 v[106:107], v22 offset:2560\n" \
    "s_waitcnt lgkmcnt(8)\n" \
    "v_mfma_f32_32x32x16_bf16 v[80:95], v[124:127], v[140:143], v[80:95]\n" \
    "ds_read_b64_tr_b16 v[108:109], v23 offset:2048\n" \
    "ds_read_b64_tr_b16 v[110:111], v23 offset:2560\n" \
    "s_waitcnt lgkmcnt(7)\n" \
    "v_mfma_f32_32x32x16_bf16 v[64:79], v[96:99], v[144:147], v[64:79]\n" \
    "s_lshl_b32 s61, s60, 13\n" \
    "v_add_u32 v20, s61, v24\n" \
    "v_xor_b32 v21, 64, v20\n" \
    "s_add_u32 s60, s60, 1\n" \
    "s_cmp_eq_u32 s60, 7\n" \
    "s_cselect_b32 s60, 0, s60\n" \
    "ds_read_b128 v[152:155], v18 offset:4096\n" \
    "ds_read_b64_tr_b16 v[112:113], v22 offset:4096\n" \
    "ds_read_b64_tr_b16 v[114:115], v22 offset:4608\n" \
    "s_waitcnt lgkmcnt(8)\n" \
    "v_mfma_f32_32x32x16_bf16 v[80:95], v[100:103], v[144:147], v[80:95]\n" \
    "ds_read_b64_tr_b16 v[116:117], v23 offset:4096\n" \
    "ds_read_b64_tr_b16 v[118:119], v23 offset:4608\n" \
    "s_waitcnt lgkmcnt(7)\n" \
    "v_mfma_f32_32x32x16_bf16 v[64:79], v[104:107], v[148:151], v[64:79]\n" \
    "ds_read_b128 v[156:159], v19 offset:4096\n" \
    "ds_read_b64_tr_b16 v[120:121], v22 offset:6144\n" \
    "ds_read_b64_tr_b16 v[122:123], v22 offset:6656\n" \
    "s_waitcnt lgkmcnt(8)\n" \
    "v_mfma_f32_32x32x16_bf16 v[80:95], v[108:111], v[148:151], v[80:95]\n" \
    "ds_read_b64_tr_b16 v[124:125], v23 offset:6144\n" \
    "ds_read_b64_tr_b16 v[126:127], v23 offset:6656\n" \
    "s_waitcnt lgkmcnt(7)\n" \
    "v_mfma_f32_32x32x16_bf16 v[64:79], v[112:115], v[152:155], v[64:79]\n" \
    "s_waitcnt lgkmcnt(4)\n" \
    "s_barrier\n" \
    "ds_read_b128 v[160:163], v16 offset:8192\n" \
    "ds_read_b64_tr_b16 v[96:97], v20 offset:0\n" \
    "ds_read_b64_tr_b16 v[98:99], v20 offset:512\n" \
    "v_mfma_f32_32x32x16_bf16 v[80:95], v[116:119], v[152:155], v[80:95]\n" \
    "ds_read_b64_tr_b16 v[100:101], v21 offset:0\n" \
    "ds_read_b64_tr_b16 v[102:103], v21 offset:512\n" \
    "s_waitcnt lgkmcnt(7)\n" \
    "v_mfma_f32_32x32x16_bf16 v[64:79], v[120:123], v[156:159], v[64:79]\n" \
    "ds_read_b128 v[164:167], v17 offset:8192\n" \
    "ds_read_b64_tr_b16 v[104:105], v20 offset:2048\n" \
    "ds_read_b64_tr_b16 v[106:107], v20 offset:2560\n" \
    "s_waitcnt lgkmcnt(8)\n" \
    "v_mfma_f32_32x32x16_bf16 v[80:95], v[124:127], v[156:159], v[80:95]\n" \
    "ds_read_b64_tr_b16 v[108:109], v21 offset:2048\n" \
    "ds_read_b64_tr_b16 v[110:111], v21 offset:2560\n" \
    "s_waitcnt lgkmcnt(7)\n" \
    "v_mfma_f32_32x32x16_bf16 v[64:79], v[96:99], v[160:163], v[64:79]\n" \
    "s_lshl_b32 s61, s60, 13\n" \
    "v_add_u32 v22, s61, v24\n" \
    "v_xor_b32 v23, 64, v22\n" \
    "s_add_u32 s60, s60, 1\n" \
    "s_cmp_eq_u32 s60, 7\n" \
    "s_cselect_b32 s60, 0, s60\n" \
    "ds_read_b128 v[168:171], v18 offset:8192\n" \
    "ds_read_b64_tr_b16 v[112:113], v20 offset:4096\n" \
    "ds_read_b64_tr_b16 v[114:115], v20 offset:4608\n" \
    "s_waitcnt lgkmcnt(8)\n" \
    "v_mfma_f32_32x32x16_bf16 v[80:95], v[100:103], v[160:163], v[80:95]\n" \
    "ds_read_b64_tr_b16 v[116:117], v21 offset:4096\n" \
    "ds_read_b64_tr_b16 v[118:119], v21 offset:4608\n" \
    "s_waitcnt lgkmcnt(7)\n" \
    "v_mfma_f32_32x32x16_bf16 v[64:79], v[104:107], v[164:167], v[64:79]\n" \
    "ds_read_b128 v[172:175], v19 offset:8192\n" \
    "ds_read_b64_tr_b16 v[120:121], v20 offset:6144\n" \
    "ds_read_b64_tr_b16 v[122:123], v20 offset:6656\n" \
    "s_waitcnt lgkmcnt(8)\n" \
    "v_mfma_f32_32x32x16_bf16 v[80:95], v[108:111], v[164:167], v[80:95]\n" \
    "ds_read_b64_tr_b16 v[124:125], v21 offset:6144\n" \
    "ds_read_b64_tr_b16 v[126:127], v21 offset:6656\n" \
    "s_waitcnt lgkmcnt(7)\n" \
    "v_mfma_f32_32x32x16_bf16 v[64:79], v[112:115], v[168:171], v[64:79]\n" \
    "s_waitcnt lgkmcnt(4)\n" \
    "s_waitcnt vmcnt(0)\n" \
    "s_barrier\n" \
    "ds_read_b128 v[176:179], v16 offset:12288\n" \
    "ds_read_b64_tr_b16 v[96:97], v22 offset:0\n" \
    "ds_read_b64_tr_b16 v[98:99], v22 offset:512\n" \
    "v_mfma_f32_32x32x16_bf16 v[80:95], v[116:119], v[168:171], v[80:95]\n" \
    "ds_read_b64_tr_b16 v[100:101], v23 offset:0\n" \
    "ds_read_b64_tr_b16 v[102:103], v23 offset:512\n" \
    "s_waitcnt lgkmcnt(7)\n" \
    "v_mfma_f32_32x32x16_bf16 v[64:79], v[120:123], v[172:175], v[64:79]\n" \
    "ds_read_b128 v[180:183], v17 offset:12288\n" \
    "ds_read_b64_tr_b16 v[104:105], v22 offset:2048\n" \
    "ds_read_b64_tr_b16 v[106:107], v22 offset:2560\n" \
    "s_waitcnt lgkmcnt(8)\n" \
    "v_mfma_f32_32x32x16_bf16 v[80:95], v[124:127], v[172:175], v[80:95]\n" \
    "ds_read_b64_tr_b16 v[108:109], v23 offset:2048\n" \
    "ds_read_b64_tr_b16 v[110:111], v23 offset:2560\n" \
    "s_waitcnt lgkmcnt(7)\n" \
    "v_mfma_f32_32x32x16_bf16 v[64:79], v[96:99], v[176:179], v[64:79]\n" \
    "s_lshl_b32 s61, s60, 13\n" \
    "v_add_u32 v20, s61, v24\n" \
    "v_xor_b32 v21, 64, v20\n" \
    "s_add_u32 s60, s60, 1\n" \
    "s_cmp_eq_u32 s60, 7\n" \
    "s_cselect_b32 s60, 0, s60\n" \
    "ds_read_b128 v[184:187], v18 offset:12288\n" \
    "ds_read_b64_tr_b16 v[112:113], v22 offset:4096\n" \
    "ds_read_b64_tr_b16 v[114:115], v22 offset:4608\n" \
    "s_waitcnt lgkmcnt(8)\n" \
    "v_mfma_f32_32x32x16_bf16 v[80:95], v[100:103], v[176:179], v[80:95]\n" \
    "ds_read_b64_tr_b16 v[116:117], v23 offset:4096\n" \
    "ds_read_b64_tr_b16 v[118:119], v23 offset:4608\n" \
    "s_waitcnt lgkmcnt(7)\n" \
    "v_mfma_f32_32x32x16_bf16 v[64:79], v[104:107], v[180:183], v[64:79]\n" \
    "ds_read_b128 v[188:191], v19 offset:12288\n" \
    "ds_read_b64_tr_b16 v[120:121], v22 offset:6144\n" \
    "ds_read_b64_tr_b16 v[122:123], v22 offset:6656\n" \
    "s_waitcnt lgkmcnt(8)\n" \
    "v_mfma_f32_32x32x16_bf16 v[80:95], v[108:111], v[180:183], v[80:95]\n" \
    "ds_read_b64_tr_b16 v[124:125], v23 offset:6144\n" \
    "ds_read_b64_tr_b16 v[126:127], v23 offset:6656\n" \
    "s_waitcnt lgkmcnt(7)\n" \
    "v_mfma_f32_32x32x16_bf16 v[64:79], v[112:115], v[184:187], v[64:79]\n" \
    "s_waitcnt lgkmcnt(4)\n" \
    "s_barrier\n" \
    "ds_read_b128 v[192:195], v16 offset:0\n" \
    "ds_read_b64_tr_b16 v[96:97], v20 offset:0\n" \
    "ds_read_b64_tr_b16 v[98:99], v20 offset:512\n" \
    "v_mfma_f32_32x32x16_bf16 v[80:95], v[116:119], v[184:187], v[80:95]\n" \
    "ds_read_b64_tr_b16 v[100:101], v21 offset:0\n" \
    "ds_read_b64_tr_b16 v[102:103], v21 offset:512\n" \
    "s_waitcnt lgkmcnt(7)\n" \
    "v_mfma_f32_32x32x16_bf16 v[64:79], v[120:123], v[188:191], v[64:79]\n" \
    "ds_read_b128 v[196:199], v17 offset:0\n" \
    "ds_read_b64_tr_b16 v[104:105], v20 offset:2048\n" \
    "ds_read_b64_tr_b16 v[106:107], v20 offset:2560\n" \
    "s_waitcnt lgkmcnt(8)\n" \
    "v_mfma_f32_32x32x16_bf16 v[80:95], v[124:127], v[188:191], v[80:95]\n" \
    "ds_read_b64_tr_b16 v[108:109], v21 offset:2048\n" \
    "ds_read_b64_tr_b16 v[110:111], v21 offset:2560\n" \
    "s_waitcnt lgkmcnt(7)\n" \
    "v_mfma_f32_32x32x16_bf16 v[64:79], v[96:99], v[192:195], v[64:79]\n" \
    "s_lshl_b32 s61, s60, 13\n" \
    "v_add_u32 v22, s61, v24\n" \
    "v_xor_b32 v23, 64, v22\n" \
    "s_add_u32 s60, s60, 1\n" \
    "s_cmp_eq_u32 s60, 7\n" \
    "s_cselect_b32 s60, 0, s60\n" \
    "ds_read_b128 v[200:203], v18 offset:0\n" \
    "ds_read_b64_tr_b16 v[112:113], v20 offset:4096\n" \
    "ds_read_b64_tr_b16 v[114:115], v20 offset:4608\n" \
    "s_waitcnt lgkmcnt(8)\n" \
    "v_mfma_f32_32x32x16_bf16 v[80:95], v[100:103], v[192:195], v[80:95]\n" \
    "ds_read_b64_tr_b16 v[116:117], v21 offset:4096\n" \
    "ds_read_b64_tr_b16 v[118:119], v21 offset:4608\n" \
    "s_waitcnt lgkmcnt(7)\n" \
    "v_mfma_f32_32x32x16_bf16 v[64:79], v[104:107], v[196:199], v[64:79]\n" \
    "ds_read_b128 v[204:207], v19 offset:0\n" \
    "ds_read_b64_tr_b16 v[120:121], v20 offset:6144\n" \
    "ds_read_b64_tr_b16 v[122:123], v20 offset:6656\n" \
    "s_waitcnt lgkmcnt(8)\n" \
    "v_mfma_f32_32x32x16_bf16 v[80:95], v[108:111], v[196:199], v[80:95]\n" \
    "ds_read_b64_tr_b16 v[124:125], v21 offset:6144\n" \
    "ds_read_b64_tr_b16 v[126:127], v21 offset:6656\n" \
    "s_waitcnt lgkmcnt(7)\n" \
    "v_mfma_f32_32x32x16_bf16 v[64:79], v[112:115], v[200:203], v[64:79]\n" \
    "s_waitcnt lgkmcnt(4)\n" \
    "s_barrier\n" \
    "ds_read_b128 v[208:211], v16 offset:4096\n" \
    "ds_read_b64_tr_b16 v[96:97], v22 offset:0\n" \
    "ds_read_b64_tr_b16 v[98:99], v22 offset:512\n" \
    "v_mfma_f32_32x32x16_bf16 v[80:95], v[116:119], v[200:203], v[80:95]\n" \
    "ds_read_b64_tr_b16 v[100:101], v23 offset:0\n" \
    "ds_read_b64_tr_b16 v[102:103], v23 offset:512\n" \
    "s_waitcnt lgkmcnt(7)\n" \
    "v_mfma_f32_32x32x16_bf16 v[64:79], v[120:123], v[204:207], v[64:79]\n" \
    "ds_read_b128 v[212:215], v17 offset:4096\n" \
    "ds_read_b64_tr_b16 v[104:105], v22 offset:2048\n" \
    "ds_read_b64_tr_b16 v[106:107], v22 offset:2560\n" \
    "s_waitcnt lgkmcnt(8)\n" \
    "v_mfma_f32_32x32x16_bf16 v[80:95], v[124:127], v[204:207], v[80:95]\n" \
    "ds_read_b64_tr_b16 v[108:109], v23 offset:2048\n" \
    "ds_read_b64_tr_b16 v[110:111], v23 offset:2560\n" \
    "s_waitcnt lgkmcnt(7)\n" \
    "v_mfma_f32_32x32x16_bf16 v[64:79], v[96:99], v[208:211], v[64:79]\n" \
    "s_lshl_b32 s61, s60, 13\n" \
    "v_add_u32 v20, s61, v24\n" \
    "v_xor_b32 v21, 64, v20\n" \
    "s_add_u32 s60, s60, 1\n" \
    "s_cmp_eq_u32 s60, 7\n" \
    "s_cselect_b32 s60, 0, s60\n" \
    "ds_read_b128 v[216:219], v18 offset:4096\n" \
    "ds_read_b64_tr_b16 v[112:113], v22 offset:4096\n" \
    "ds_read_b64_tr_b16 v[114:115], v22 offset:4608\n" \
    "s_waitcnt lgkmcnt(8)\n" \
    "v_mfma_f32_32x32x16_bf16 v[80:95], v[100:103], v[208:211], v[80:95]\n" \
    "ds_read_b64_tr_b16 v[116:117], v23 offset:4096\n" \
    "ds_read_b64_tr_b16 v[118:119], v23 offset:4608\n" \
    "s_waitcnt lgkmcnt(7)\n" \
    "v_mfma_f32_32x32x16_bf16 v[64:79], v[104:107], v[212:215], v[64:79]\n" \
    "ds_read_b128 v[220:223], v19 offset:4096\n" \
    "ds_read_b64_tr_b16 v[120:121], v22 offset:6144\n" \
    "ds_read_b64_tr_b16 v[122:123], v22 offset:6656\n" \
    "s_waitcnt lgkmcnt(8)\n" \
    "v_mfma_f32_32x32x16_bf16 v[80:95], v[108:111], v[212:215], v[80:95]\n" \
    "ds_read_b64_tr_b16 v[124:125], v23 offset:6144\n" \
    "ds_read_b64_tr_b16 v[126:127], v23 offset:6656\n" \
    "s_waitcnt lgkmcnt(7)\n" \
    "v_mfma_f32_32x32x16_bf16 v[64:79], v[112:115], v[216:219], v[64:79]\n" \
    "s_waitcnt lgkmcnt(4)\n" \
    "s_barrier\n" \
    "ds_read_b128 v[224:227], v16 offset:8192\n" \
    "ds_read_b64_tr_b16 v[96:97], v20 offset:0\n" \
    "ds_read_b64_tr_b16 v[98:99], v20 offset:512\n" \
    "v_mfma_f32_32x32x16_bf16 v[80:95], v[116:119], v[216:219], v[80:95]\n" \
    "ds_read_b64_tr_b16 v[100:101], v21 offset:0\n" \
    "ds_read_b64_tr_b16 v[102:103], v21 offset:512\n" \
    "s_waitcnt lgkmcnt(7)\n" \
    "v_mfma_f32_32x32x16_bf16 v[64:79], v[120:123], v[220:223], v[64:79]\n" \
    "ds_read_b128 v[228:231], v17 offset:8192\n" \
    "ds_read_b64_tr_b16 v[104:105], v20 offset:2048\n" \
    "ds_read_b64_tr_b16 v[106:107], v20 offset:2560\n" \
    "s_waitcnt lgkmcnt(8)\n" \
    "v_mfma_f32_32x32x16_bf16 v[80:95], v[124:127], v[220:223], v[80:95]\n" \
    "ds_read_b64_tr_b16 v[108:109], v21 offset:2048\n" \
    "ds_read_b64_tr_b16 v[110:111], v21 offset:2560\n" \
    "s_waitcnt lgkmcnt(7)\n" \
    "v_mfma_f32_32x32x16_bf16 v[64:79], v[96:99], v[224:227], v[64:79]\n" \
    "ds_read_b128 v[232:235], v18 offset:8192\n" \
    "ds_read_b64_tr_b16 v[112:113], v20 offset:4096\n" \
    "ds_read_b64_tr_b16 v[114:115], v20 offset:4608\n" \
    "s_waitcnt lgkmcnt(8)\n" \
    "v_mfma_f32_32x32x16_bf16 v[80:95], v[100:103], v[224:227], v[80:95]\n" \
    "ds_read_b64_tr_b16 v[116:117], v21 offset:4096\n" \
    "ds_read_b64_tr_b16 v[118:119], v21 offset:4608\n" \
    "s_waitcnt lgkmcnt(7)\n" \
    "v_mfma_f32_32x32x16_bf16 v[64:79], v[104:107], v[228:231], v[64:79]\n" \
    "ds_read_b128 v[236:239], v19 offset:8192\n" \
    "ds_read_b64_tr_b16 v[120:121], v20 offset:6144\n" \
    "ds_read_b64_tr_b16 v[122:123], v20 offset:6656\n" \
    "s_waitcnt lgkmcnt(8)\n" \
    "v_mfma_f32_32x32x16_bf16 v[80:95], v[108:111], v[228:231], v[80:95]\n" \
    "ds_read_b64_tr_b16 v[124:125], v21 offset:6144\n" \
    "ds_read_b64_tr_b16 v[126:127], v21 offset:6656\n" \
    "s_waitcnt lgkmcnt(7)\n" \
    "v_mfma_f32_32x32x16_bf16 v[64:79], v[112:115], v[232:235], v[64:79]\n" \
    "s_waitcnt lgkmcnt(5)\n" \
    "v_mfma_f32_32x32x16_bf16 v[80:95], v[116:119], v[232:235], v[80:95]\n" \
    "s_waitcnt lgkmcnt(2)\n" \
    "v_mfma_f32_32x32x16_bf16 v[64:79], v[120:123], v[236:239], v[64:79]\n" \
    "s_waitcnt lgkmcnt(0)\n" \
    "v_mfma_f32_32x32x16_bf16 v[80:95], v[124:127], v[236:239], v[80:95]\n" \
    "s_lshl_b32 s61, %[t], 2\n" \
    "s_add_u32 s61, s61, %[wv]\n" \
    "s_lshl_b32 s61, s61, 12\n" \
    "s_setprio 3\n" \
    "buffer_store_dwordx4 v[4:7], v12, %[rws], 0 offen sc1\n" \
    "s_nop 5\n" \
    "v_cvt_pk_bf16_f32 v96, v64, v65\n" \
    "v_cvt_pk_bf16_f32 v97, v66, v67\n" \
    "buffer_store_dwordx2 v[96:97], v32, %[rkv], s61 offen sc1\n" \
    "v_cvt_pk_bf16_f32 v98, v68, v69\n" \
    "v_cvt_pk_bf16_f32 v99, v70, v71\n" \
    "buffer_store_dwordx2 v[98:99], v33, %[rkv], s61 offen sc1\n" \
    "v_cvt_pk_bf16_f32 v100, v72, v73\n" \
    "v_cvt_pk_bf16_f32 v101, v74, v75\n" \
    "buffer_store_dwordx2 v[100:101], v34, %[rkv], s61 offen sc1\n" \
    "v_cvt_pk_bf16_f32 v102, v76, v77\n" \
    "v_cvt_pk_bf16_f32 v103, v78, v79\n" \
    "buffer_store_dwordx2 v[102:103], v35, %[rkv], s61 offen sc1\n" \
    "v_cvt_pk_bf16_f32 v104, v80, v81\n" \
    "v_cvt_pk_bf16_f32 v105, v82, v83\n" \
    "buffer_store_dwordx2 v[104:105], v36, %[rkv], s61 offen sc1\n" \
    "v_cvt_pk_bf16_f32 v106, v84, v85\n" \
    "v_cvt_pk_bf16_f32 v107, v86, v87\n" \
    "buffer_store_dwordx2 v[106:107], v37, %[rkv], s61 offen sc1\n" \
    "v_cvt_pk_bf16_f32 v108, v88, v89\n" \
    "v_cvt_pk_bf16_f32 v109, v90, v91\n" \
    "buffer_store_dwordx2 v[108:109], v38, %[rkv], s61 offen sc1\n" \
    "v_cvt_pk_bf16_f32 v110, v92, v93\n" \
    "v_cvt_pk_bf16_f32 v111, v94, v95\n" \
    "buffer_store_dwordx2 v[110:111], v39, %[rkv], s61 offen sc1\n" \
    "s_setprio 0\n" \
    "s_branch MN_RDONE_%=\n" \
    "MN_NOTK_%=:\n" \
    "s_cmp_lt_u32 %[idx], 14\n" \
    "s_cbranch_scc0 MN_SPARE_%=\n" \
    "s_lshl_b32 s61, s60, 13\n" \
    "v_add_u32 v20, s61, v24\n" \
    "v_xor_b32 v21, 64, v20\n" \
    "s_add_u32 s60, s60, 1\n" \
    "s_cmp_eq_u32 s60, 7\n" \
    "s_cselect_b32 s60, 0, s60\n" \
    "s_barrier\n" \
    "ds_read_b128 v[128:131], v16 offset:0\n" \
    "ds_read_b64_tr_b16 v[96:97], v20 offset:0\n" \
    "ds_read_b64_tr_b16 v[98:99], v20 offset:512\n" \
    "ds_read_b64_tr_b16 v[100:101], v21 offset:0\n" \
    "ds_read_b64_tr_b16 v[102:103], v21 offset:512\n" \
    "ds_read_b128 v[132:135], v17 offset:0\n" \
    "ds_read_b64_tr_b16 v[104:105], v20 offset:2048\n" \
    "ds_read_b64_tr_b16 v[106:107], v20 offset:2560\n" \
    "ds_read_b64_tr_b16 v[108:109], v21 offset:2048\n" \
    "ds_read_b64_tr_b16 v[110:111], v21 offset:2560\n" \
    "s_waitcnt lgkmcnt(7)\n" \
    "v_mfma_f32_32x32x16_bf16 v[64:79], v[128:131], v[96:99], 0\n" \
    "s_lshl_b32 s61, s60, 13\n" \
    "v_add_u32 v22, s61, v24\n" \
    "v_xor_b32 v23, 64, v22\n" \
    "s_add_u32 s60, s60, 1\n" \
    "s_cmp_eq_u32 s60, 7\n" \
    "s_cselect_b32 s60, 0, s60\n" \
    "ds_read_b128 v[136:139], v18 offset:0\n" \
    "ds_read_b64_tr_b16 v[112:113], v20 offset:4096\n" \
    "ds_read_b64_tr_b16 v[114:115], v20 offset:4608\n" \
    "s_waitcnt lgkmcnt(8)\n" \
    "v_mfma_f32_32x32x16_bf16 v[80:95], v[128:131], v[100:103], 0\n" \
    "ds_read_b64_tr_b16 v[116:117], v21 offset:4096\n" \
    "ds_read_b64_tr_b16 v[118:119], v21 offset:4608\n" \
    "s_waitcnt lgkmcnt(7)\n" \
    "v_mfma_f32_32x32x16_bf16 v[64:79], v[132:135], v[104:107], v[64:79]\n" \
    "ds_read_b128 v[140:143], v19 offset:0\n" \
    "ds_read_b64_tr_b16 v[120:121], v20 offset:6144\n" \
    "ds_read_b64_tr_b16 v[122:123], v20 offset:6656\n" \
    "s_waitcnt lgkmcnt(8)\n" \
    "v_mfma_f32_32x32x16_bf16 v[80:95], v[132:135], v[108:111], v[80:95]\n" \
    "ds_read_b64_tr_b16 v[124:125], v21 offset:6144\n" \
    "ds_read_b64_tr_b16 v[126:127], v21 offset:6656\n" \
    "s_waitcnt lgkmcnt(7)\n" \
    "v_mfma_f32_32x32x16_bf16 v[64:79], v[136:139], v[112:115], v[64:79]\n" \
    "s_waitcnt lgkmcnt(4)\n" \
    "s_barrier\n" \
    "ds_read_b128 v[144:147], v16 offset:4096\n" \
    "ds_read_b64_tr_b16 v[96:97], v22 offset:0\n" \
    "ds_read_b64_tr_b16 v[98:99], v22 offset:512\n" \
    "v_mfma_f32_32x32x16_bf16 v[80:95], v[136:139], v[116:119], v[80:95]\n" \
    "ds_read_b64_tr_b16 v[100:101], v23 offset:0\n" \
    "ds_read_b64_tr_b16 v[102:103], v23 offset:512\n" \
    "s_waitcnt lgkmcnt(7)\n" \
    "v_mfma_f32_32x32x16_bf16 v[64:79], v[140:143], v[120:123], v[64:79]\n" \
    "ds_read_b128 v[148:151], v17 offset:4096\n" \
    "ds_read_b64_tr_b16 v[104:105], v22 offset:2048\n" \
    "ds_read_b64_tr_b16 v[106:107], v22 offset:2560\n" \
    "s_waitcnt lgkmcnt(8)\n" \
    "v_mfma_f32_32x32x16_bf16 v[80:95], v[140:143], v[124:127], v[80:95]\n" \
    "ds_read_b64_tr_b16 v[108:109], v23 offset:2048\n" \
    "ds_read_b64_tr_b16 v[110:111], v23 offset:2560\n" \
    "s_waitcnt lgkmcnt(7)\n" \
    "v_mfma_f32_32x32x16_bf16 v[64:79], v[144:147], v[96:99], v[64:79]\n" \
    "s_lshl_b32 s61, s60, 13\n" \
    "v_add_u32 v20, s61, v24\n" \
    "v_xor_b32 v21, 64, v20\n" \
    "s_add_u32 s60, s60, 1\n" \
    "s_cmp_eq_u32 s60, 7\n" \
    "s_cselect_b32 s60, 0, s60\n" \
    "ds_read_b128 v[152:155], v18 offset:4096\n" \
    "ds_read_b64_tr_b16 v[112:113], v22 offset:4096\n" \
    "ds_read_b64_tr_b16 v[114:115], v22 offset:4608\n" \
    "s_waitcnt lgkmcnt(8)\n" \
    "v_mfma_f32_32x32x16_bf16 v[80:95], v[144:147], v[100:103], v[80:95]\n" \
    "ds_read_b64_tr_b16 v[116:117], v23 offset:4096\n" \
    "ds_read_b64_tr_b16 v[118:119], v23 offset:4608\n" \
    "s_waitcnt lgkmcnt(7)\n" \
    "v_mfma_f32_32x32x16_bf16 v[64:79], v[148:151], v[104:107], v[64:79]\n" \
    "ds_read_b128 v[156:159], v19 offset:4096\n" \
    "ds_read_b64_tr_b16 v[120:121], v22 offset:6144\n" \
    "ds_read_b64_tr_b16 v[122:123], v22 offset:6656\n" \
    "s_waitcnt lgkmcnt(8)\n" \
    "v_mfma_f32_32x32x16_bf16 v[80:95], v[148:151], v[108:111], v[80:95]\n" \
    "ds_read_b64_tr_b16 v[124:125], v23 offset:6144\n" \
    "ds_read_b64_tr_b16 v[126:127], v23 offset:6656\n" \
    "s_waitcnt lgkmcnt(7)\n" \
    "v_mfma_f32_32x32x16_bf16 v[64:79], v[152:155], v[112:115], v[64:79]\n" \
    "s_waitcnt lgkmcnt(4)\n" \
    "s_barrier\n" \
    "ds_read_b128 v[160:163], v16 offset:8192\n" \
    "ds_read_b64_tr_b16 v[96:97], v20 offset:0\n" \
    "ds_read_b64_tr_b16 v[98:99], v20 offset:512\n" \
    "v_mfma_f32_32x32x16_bf16 v[80:95], v[152:155], v[116:119], v[80:95]\n" \
    "ds_read_b64_tr_b16 v[100:101], v21 offset:0\n" \
    "ds_read_b64_tr_b16 v[102:103], v21 offset:512\n" \
    "s_waitcnt lgkmcnt(7)\n" \
    "v_mfma_f32_32x32x16_bf16 v[64:79], v[156:159], v[120:123], v[64:79]\n" \
    "ds_read_b128 v[164:167], v17 offset:8192\n" \
    "ds_read_b64_tr_b16 v[104:105], v20 offset:2048\n" \
    "ds_read_b64_tr_b16 v[106:107], v20 offset:2560\n" \
    "s_waitcnt lgkmcnt(8)\n" \
    "v_mfma_f32_32x32x16_bf16 v[80:95], v[156:159], v[124:127], v[80:95]\n" \
    "ds_read_b64_tr_b16 v[108:109], v21 offset:2048\n" \
    "ds_read_b64_tr_b16 v[110:111], v21 offset:2560\n" \
    "s_waitcnt lgkmcnt(7)\n" \
    "v_mfma_f32_32x32x16_bf16 v[64:79], v[160:163], v[96:99], v[64:79]\n" \
    "s_lshl_b32 s61, s60, 13\n" \
    "v_add_u32 v22, s61, v24\n" \
    "v_xor_b32 v23, 64, v22\n" \
    "s_add_u32 s60, s60, 1\n" \
    "s_cmp_eq_u32 s60, 7\n" \
    "s_cselect_b32 s60, 0, s60\n" \
    "ds_read_b128 v[168:171], v18 offset:8192\n" \
    "ds_read_b64_tr_b16 v[112:113], v20 offset:4096\n" \
    "ds_read_b64_tr_b16 v[114:115], v20 offset:4608\n" \
    "s_waitcnt lgkmcnt(8)\n" \
    "v_mfma_f32_32x32x16_bf16 v[80:95], v[160:163], v[100:103], v[80:95]\n" \
    "ds_read_b64_tr_b16 v[116:117], v21 offset:4096\n" \
    "ds_read_b64_tr_b16 v[118:119], v21 offset:4608\n" \
    "s_waitcnt lgkmcnt(7)\n" \
    "v_mfma_f32_32x32x16_bf16 v[64:79], v[164:167], v[104:107], v[64:79]\n" \
    "ds_read_b128 v[172:175], v19 offset:8192\n" \
    "ds_read_b64_tr_b16 v[120:121], v20 offset:6144\n" \
    "ds_read_b64_tr_b16 v[122:123], v20 offset:6656\n" \
    "s_waitcnt lgkmcnt(8)\n" \
    "v_mfma_f32_32x32x16_bf16 v[80:95], v[164:167], v[108:111], v[80:95]\n" \
    "ds_read_b64_tr_b16 v[124:125], v21 offset:6144\n" \
    "ds_read_b64_tr_b16 v[126:127], v21 offset:6656\n" \
    "s_waitcnt lgkmcnt(7)\n" \
    "v_mfma_f32_32x32x16_bf16 v[64:79], v[168:171], v[112:115], v[64:79]\n" \
    "s_waitcnt lgkmcnt(4)\n" \
    "s_waitcnt vmcnt(0)\n" \
    "s_barrier\n" \
    "ds_read_b128 v[176:179], v16 offset:12288\n" \
    "ds_read_b64_tr_b16 v[96:97], v22 offset:0\n" \
    "ds_read_b64_tr_b16 v[98:99], v22 offset:512\n" \
    "v_mfma_f32_32x32x16_bf16 v[80:95], v[168:171], v[116:119], v[80:95]\n" \
    "ds_read_b64_tr_b16 v[100:101], v23 offset:0\n" \
    "ds_read_b64_tr_b16 v[102:103], v23 offset:512\n" \
    "s_waitcnt lgkmcnt(7)\n" \
    "v_mfma_f32_32x32x16_bf16 v[64:79], v[172:175], v[120:123], v[64:79]\n" \
    "ds_read_b128 v[180:183], v17 offset:12288\n" \
    "ds_read_b64_tr_b16 v[104:105], v22 offset:2048\n" \
    "ds_read_b64_tr_b16 v[106:107], v22 offset:2560\n" \
    "s_waitcnt lgkmcnt(8)\n" \
    "v_mfma_f32_32x32x16_bf16 v[80:95], v[172:175], v[124:127], v[80:95]\n" \
    "ds_read_b64_tr_b16 v[108:109], v23 offset:2048\n" \
    "ds_read_b64_tr_b16 v[110:111], v23 offset:2560\n" \
    "s_waitcnt lgkmcnt(7)\n" \
    "v_mfma_f32_32x32x16_bf16 v[64:79], v[176:179], v[96:99], v[64:79]\n" \
    "s_lshl_b32 s61, s60, 13\n" \
    "v_add_u32 v20, s61, v24\n" \
    "v_xor_b32 v21, 64, v20\n" \
    "s_add_u32 s60, s60, 1\n" \
    "s_cmp_eq_u32 s60, 7\n" \
    "s_cselect_b32 s60, 0, s60\n" \
    "ds_read_b128 v[184:187], v18 offset:12288\n" \
    "ds_read_b64_tr_b16 v[112:113], v22 offset:4096\n" \
    "ds_read_b64_tr_b16 v[114:115], v22 offset:4608\n" \
    "s_waitcnt lgkmcnt(8)\n" \
    "v_mfma_f32_32x32x16_bf16 v[80:95], v[176:179], v[100:103], v[80:95]\n" \
    "ds_read_b64_tr_b16 v[116:117], v23 offset:4096\n" \
    "ds_read_b64_tr_b16 v[118:119], v23 offset:4608\n" \
    "s_waitcnt lgkmcnt(7)\n" \
    "v_mfma_f32_32x32x16_bf16 v[64:79], v[180:183], v[104:107], v[64:79]\n" \
    "ds_read_b128 v[188:191], v19 offset:12288\n" \
    "ds_read_b64_tr_b16 v[120:121], v22 offset:6144\n" \
    "ds_read_b64_tr_b16 v[122:123], v22 offset:6656\n" \
    "s_waitcnt lgkmcnt(8)\n" \
    "v_mfma_f32_32x32x16_bf16 v[80:95], v[180:183], v[108:111], v[80:95]\n" \
    "ds_read_b64_tr_b16 v[124:125], v23 offset:6144\n" \
    "ds_read_b64_tr_b16 v[126:127], v23 offset:6656\n" \
    "s_waitcnt lgkmcnt(7)\n" \
    "v_mfma_f32_32x32x16_bf16 v[64:79], v[184:187], v[112:115], v[64:79]\n" \
    "s_waitcnt lgkmcnt(4)\n" \
    "s_barrier\n" \
    "ds_read_b128 v[192:195], v16 offset:0\n" \
    "ds_read_b64_tr_b16 v[96:97], v20 offset:0\n" \
    "ds_read_b64_tr_b16 v[98:99], v20 offset:512\n" \
    "v_mfma_f32_32x32x16_bf16 v[80:95], v[184:187], v[116:119], v[80:95]\n" \
    "ds_read_b64_tr_b16 v[100:101], v21 offset:0\n" \
    "ds_read_b64_tr_b16 v[102:103], v21 offset:512\n" \
    "s_waitcnt lgkmcnt(7)\n" \
    "v_mfma_f32_32x32x16_bf16 v[64:79], v[188:191], v[120:123], v[64:79]\n" \
    "ds_read_b128 v[196:199], v17 offset:0\n" \
    "ds_read_b64_tr_b16 v[104:105], v20 offset:2048\n" \
    "ds_read_b64_tr_b16 v[106:107], v20 offset:2560\n" \
    "s_waitcnt lgkmcnt(8)\n" \
    "v_mfma_f32_32x32x16_bf16 v[80:95], v[188:191], v[124:127], v[80:95]\n" \
    "ds_read_b64_tr_b16 v[108:109], v21 offset:2048\n" \
    "ds_read_b64_tr_b16 v[110:111], v21 offset:2560\n" \
    "s_waitcnt lgkmcnt(7)\n" \
    "v_mfma_f32_32x32x16_bf16 v[64:79], v[192:195], v[96:99], v[64:79]\n" \
    "s_lshl_b32 s61, s60, 13\n" \
    "v_add_u32 v22, s61, v24\n" \
    "v_xor_b32 v23, 64, v22\n" \
    "s_add_u32 s60, s60, 1\n" \
    "s_cmp_eq_u32 s60, 7\n" \
    "s_cselect_b32 s60, 0, s60\n" \
    "ds_read_b128 v[200:203], v18 offset:0\n" \
    "ds_read_b64_tr_b16 v[112:113], v20 offset:4096\n" \
    "ds_read_b64_tr_b16 v[114:115], v20 offset:4608\n" \
    "s_waitcnt lgkmcnt(8)\n" \
    "v_mfma_f32_32x32x16_bf16 v[80:95], v[192:195], v[100:103], v[80:95]\n" \
    "ds_read_b64_tr_b16 v[116:117], v21 offset:4096\n" \
    "ds_read_b64_tr_b16 v[118:119], v21 offset:4608\n" \
    "s_waitcnt lgkmcnt(7)\n" \
    "v_mfma_f32_32x32x16_bf16 v[64:79], v[196:199], v[104:107], v[64:79]\n" \
    "ds_read_b128 v[204:207], v19 offset:0\n" \
    "ds_read_b64_tr_b16 v[120:121], v20 offset:6144\n" \
    "ds_read_b64_tr_b16 v[122:123], v20 offset:6656\n" \
    "s_waitcnt lgkmcnt(8)\n" \
    "v_mfma_f32_32x32x16_bf16 v[80:95], v[196:199], v[108:111], v[80:95]\n" \
    "ds_read_b64_tr_b16 v[124:125], v21 offset:6144\n" \
    "ds_read_b64_tr_b16 v[126:127], v21 offset:6656\n" \
    "s_waitcnt lgkmcnt(7)\n" \
    "v_mfma_f32_32x32x16_bf16 v[64:79], v[200:203], v[112:115], v[64:79]\n" \
    "s_waitcnt lgkmcnt(4)\n" \
    "s_barrier\n" \
    "ds_read_b128 v[208:211], v16 offset:4096\n" \
    "ds_read_b64_tr_b16 v[96:97], v22 offset:0\n" \
    "ds_read_b64_tr_b16 v[98:99], v22 offset:512\n" \
    "v_mfma_f32_32x32x16_bf16 v[80:95], v[200:203], v[116:119], v[80:95]\n" \
    "ds_read_b64_tr_b16 v[100:101], v23 offset:0\n" \
    "ds_read_b64_tr_b16 v[102:103], v23 offset:512\n" \
    "s_waitcnt lgkmcnt(7)\n" \
    "v_mfma_f32_32x32x16_bf16 v[64:79], v[204:207], v[120:123], v[64:79]\n" \
    "ds_read_b128 v[212:215], v17 offset:4096\n" \
    "ds_read_b64_tr_b16 v[104:105], v22 offset:2048\n" \
    "ds_read_b64_tr_b16 v[106:107], v22 offset:2560\n" \
    "s_waitcnt lgkmcnt(8)\n" \
    "v_mfma_f32_32x32x16_bf16 v[80:95], v[204:207], v[124:127], v[80:95]\n" \
    "ds_read_b64_tr_b16 v[108:109], v23 offset:2048\n" \
    "ds_read_b64_tr_b16 v[110:111], v23 offset:2560\n" \
    "s_waitcnt lgkmcnt(7)\n" \
    "v_mfma_f32_32x32x16_bf16 v[64:79], v[208:211], v[96:99], v[64:79]\n" \
    "s_lshl_b32 s61, s60, 13\n" \
    "v_add_u32 v20, s61, v24\n" \
    "v_xor_b32 v21, 64, v20\n" \
    "s_add_u32 s60, s60, 1\n" \
    "s_cmp_eq_u32 s60, 7\n" \
    "s_cselect_b32 s60, 0, s60\n" \
    "ds_read_b128 v[216:219], v18 offset:4096\n" \
    "ds_read_b64_tr_b16 v[112:113], v22 offset:4096\n" \
    "ds_read_b64_tr_b16 v[114:115], v22 offset:4608\n" \
    "s_waitcnt lgkmcnt(8)\n" \
    "v_mfma_f32_32x32x16_bf16 v[80:95], v[208:211], v[100:103], v[80:95]\n" \
    "ds_read_b64_tr_b16 v[116:117], v23 offset:4096\n" \
    "ds_read_b64_tr_b16 v[118:119], v23 offset:4608\n" \
    "s_waitcnt lgkmcnt(7)\n" \
    "v_mfma_f32_32x32x16_bf16 v[64:79], v[212:215], v[104:107], v[64:79]\n" \
    "ds_read_b128 v[220:223], v19 offset:4096\n" \
    "ds_read_b64_tr_b16 v[120:121], v22 offset:6144\n" \
    "ds_read_b64_tr_b16 v[122:123], v22 offset:6656\n" \
    "s_waitcnt lgkmcnt(8)\n" \
    "v_mfma_f32_32x32x16_bf16 v[80:95], v[212:215], v[108:111], v[80:95]\n" \
    "ds_read_b64_tr_b16 v[124:125], v23 offset:6144\n" \
    "ds_read_b64_tr_b16 v[126:127], v23 offset:6656\n" \
    "s_waitcnt lgkmcnt(7)\n" \
    "v_mfma_f32_32x32x16_bf16 v[64:79], v[216:219], v[112:115], v[64:79]\n" \
    "s_waitcnt lgkmcnt(4)\n" \
    "s_barrier\n" \
    "ds_read_b128 v[224:227], v16 offset:8192\n" \
    "ds_read_b64_tr_b16 v[96:97], v20 offset:0\n" \
    "ds_read_b64_tr_b16 v[98:99], v20 offset:512\n" \
    "v_mfma_f32_32x32x16_bf16 v[80:95], v[216:219], v[116:119], v[80:95]\n" \
    "ds_read_b64_tr_b16 v[100:101], v21 offset:0\n" \
    "ds_read_b64_tr_b16 v[102:103], v21 offset:512\n" \
    "s_waitcnt lgkmcnt(7)\n" \
    "v_mfma_f32_32x32x16_bf16 v[64:79], v[220:223], v[120:123], v[64:79]\n" \
    "ds_read_b128 v[228:231], v17 offset:8192\n" \
    "ds_read_b64_tr_b16 v[104:105], v20 offset:2048\n" \
    "ds_read_b64_tr_b16 v[106:107], v20 offset:2560\n" \
    "s_waitcnt lgkmcnt(8)\n" \
    "v_mfma_f32_32x32x16_bf16 v[80:95], v[220:223], v[124:127], v[80:95]\n" \
    "ds_read_b64_tr_b16 v[108:109], v21 offset:2048\n" \
    "ds_read_b64_tr_b16 v[110:111], v21 offset:2560\n" \
    "s_waitcnt lgkmcnt(7)\n" \
    "v_mfma_f32_32x32x16_bf16 v[64:79], v[224:227], v[96:99], v[64:79]\n" \
    "ds_read_b128 v[232:235], v18 offset:8192\n" \
    "ds_read_b64_tr_b16 v[112:113], v20 offset:4096\n" \
    "ds_read_b64_tr_b16 v[114:115], v20 offset:4608\n" \
    "s_waitcnt lgkmcnt(8)\n" \
    "v_mfma_f32_32x32x16_bf16 v[80:95], v[224:227], v[100:103], v[80:95]\n" \
    "ds_read_b64_tr_b16 v[116:117], v21 offset:4096\n" \
    "ds_read_b64_tr_b16 v[118:119], v21 offset:4608\n" \
    "s_waitcnt lgkmcnt(7)\n" \
    "v_mfma_f32_32x32x16_bf16 v[64:79], v[228:231], v[104:107], v[64:79]\n" \
    "ds_read_b128 v[236:239], v19 offset:8192\n" \
    "ds_read_b64_tr_b16 v[120:121], v20 offset:6144\n" \
    "ds_read_b64_tr_b16 v[122:123], v20 offset:6656\n" \
    "s_waitcnt lgkmcnt(8)\n" \
    "v_mfma_f32_32x32x16_bf16 v[80:95], v[228:231], v[108:111], v[80:95]\n" \
    "ds_read_b64_tr_b16 v[124:125], v21 offset:6144\n" \
    "ds_read_b64_tr_b16 v[126:127], v21 offset:6656\n" \
    "s_waitcnt lgkmcnt(7)\n" \
    "v_mfma_f32_32x32x16_bf16 v[64:79], v[232:235], v[112:115], v[64:79]\n" \
    "s_waitcnt lgkmcnt(5)\n" \
    "v_mfma_f32_32x32x16_bf16 v[80:95], v[232:235], v[116:119], v[80:95]\n" \
    "s_waitcnt lgkmcnt(2)\n" \
    "v_mfma_f32_32x32x16_bf16 v[64:79], v[236:239], v[120:123], v[64:79]\n" \
    "s_waitcnt lgkmcnt(0)\n" \
    "v_mfma_f32_32x32x16_bf16 v[80:95], v[236:239], v[124:127], v[80:95]\n" \
    "s_lshl_b32 s61, %[t], 2\n" \
    "s_add_u32 s61, s61, %[wv]\n" \
    "s_lshl_b32 s61, s61, 12\n" \
    "s_setprio 3\n" \
    "buffer_store_dwordx4 v[4:7], v12, %[rws], 0 offen sc1\n" \
    "s_nop 5\n" \
    "v_cvt_pk_bf16_f32 v96, v64, v65\n" \
    "v_cvt_pk_bf16_f32 v97, v66, v67\n" \
    "v_cvt_pk_bf16_f32 v98, v68, v69\n" \
    "v_cvt_pk_bf16_f32 v99, v70, v71\n" \
    "buffer_store_dwordx4 v[96:99], v32, %[rkv], s61 offen offset:0 sc1\n" \
    "v_cvt_pk_bf16_f32 v100, v72, v73\n" \
    "v_cvt_pk_bf16_f32 v101, v74, v75\n" \
    "v_cvt_pk_bf16_f32 v102, v76, v77\n" \
    "v_cvt_pk_bf16_f32 v103, v78, v79\n" \
    "buffer_store_dwordx4 v[100:103], v32, %[rkv], s61 offen offset:1024 sc1\n" \
    "v_cvt_pk_bf16_f32 v104, v80, v81\n" \
    "v_cvt_pk_bf16_f32 v105, v82, v83\n" \
    "v_cvt_pk_bf16_f32 v106, v84, v85\n" \
    "v_cvt_pk_bf16_f32 v107, v86, v87\n" \
    "buffer_store_dwordx4 v[104:107], v33, %[rkv], s61 offen offset:0 sc1\n" \
    "v_cvt_pk_bf16_f32 v108, v88, v89\n" \
    "v_cvt_pk_bf16_f32 v109, v90, v91\n" \
    "v_cvt_pk_bf16_f32 v110, v92, v93\n" \
    "v_cvt_pk_bf16_f32 v111, v94, v95\n" \
    "buffer_store_dwordx4 v[108:111], v33, %[rkv], s61 offen offset:1024 sc1\n" \
    "s_setprio 0\n" \
    "s_branch MN_RDONE_%=\n" \
    "MN_SPARE_%=:\n" \
    "s_waitcnt lgkmcnt(0)\n" \
    "s_barrier\n" \
    "ds_read_b128 v[128:131], v16 offset:0\n" \
    "ds_read_b128 v[132:135], v17 offset:0\n" \
    "ds_read_b128 v[136:139], v18 offset:0\n" \
    "ds_read_b128 v[140:143], v19 offset:0\n" \
    "s_waitcnt lgkmcnt(0)\n" \
    "s_barrier\n" \
    "ds_read_b128 v[144:147], v16 offset:4096\n" \
    "ds_read_b128 v[148:151], v17 offset:4096\n" \
    "ds_read_b128 v[152:155], v18 offset:4096\n" \
    "ds_read_b128 v[156:159], v19 offset:4096\n" \
    "s_waitcnt lgkmcnt(0)\n" \
    "s_barrier\n" \
    "ds_read_b128 v[160:163], v16 offset:8192\n" \
    "ds_read_b128 v[164:167], v17 offset:8192\n" \
    "ds_read_b128 v[168:171], v18 offset:8192\n" \
    "ds_read_b128 v[172:175], v19 offset:8192\n" \
    "s_waitcnt lgkmcnt(0)\n" \
    "s_waitcnt vmcnt(0)\n" \
    "s_barrier\n" \
    "ds_read_b128 v[176:179], v16 offset:12288\n" \
    "ds_read_b128 v[180:183], v17 offset:12288\n" \
    "ds_read_b128 v[184:187], v18 offset:12288\n" \
    "ds_read_b128 v[188:191], v19 offset:12288\n" \
    "s_waitcnt lgkmcnt(0)\n" \
    "s_barrier\n" \
    "ds_read_b128 v[192:195], v16 offset:0\n" \
    "ds_read_b128 v[196:199], v17 offset:0\n" \
    "ds_read_b128 v[200:203], v18 offset:0\n" \
    "ds_read_b128 v[204:207], v19 offset:0\n" \
    "s_waitcnt lgkmcnt(0)\n" \
    "s_barrier\n" \
    "ds_read_b128 v[208:211], v16 offset:4096\n" \
    "ds_read_b128 v[212:215], v17 offset:4096\n" \
    "ds_read_b128 v[216:219], v18 offset:4096\n" \
    "ds_read_b128 v[220:223], v19 offset:4096\n" \
    "s_waitcnt lgkmcnt(0)\n" \
    "s_barrier\n" \
    "ds_read_b128 v[224:227], v16 offset:8192\n" \
    "ds_read_b128 v[228:231], v17 offset:8192\n" \
    "ds_read_b128 v[232:235], v18 offset:8192\n" \
    "ds_read_b128 v[236:239], v19 offset:8192\n" \
    "s_waitcnt lgkmcnt(0)\n" \
    "MN_RDONE_%=:\n" \
    "ds_read_b64 v[40:41], v3\n" \
    "s_mov_b32 s60, %[rot]\n" \
    "s_lshl_b32 s61, s60, 12\n" \
    "v_add_u32 v20, s61, v25\n" \
    "s_waitcnt lgkmcnt(0)\n" \
    "s_barrier\n" \
    "v_readfirstlane_b32 s64, v40\n" \
    "v_readfirstlane_b32 s65, v41\n" \
    "v_mov_b32 v9, 1\n" \
    "s_add_u32 s64, s64, 1\n" \
    "s_addc_u32 s65, s65, 0\n" \
    "s_waitcnt vmcnt(0)\n" \
    "s_mov_b64 exec, 1\n" \
    "ds_add_rtn_u32 v9, v3, v9 offset:28\n" \
    "s_waitcnt lgkmcnt(0)\n" \
    "v_readfirstlane_b32 s61, v9\n" \
    "s_mov_b64 exec, -1\n" \
    "s_cmp_lg_u32 s61, 3\n" \
    "s_cbranch_scc1 MN_NOF1_%=\n" \
    "s_lshl_b32 s61, %[t], 4\n" \
    "s_add_u32 s61, s61, %[idx]\n" \
    "s_lshl_b32 s61, s61, 7\n" \
    "s_add_u32 s61, s61, 4096\n" \
    "s_mov_b64 exec, 1\n" \
    "v_mov_b32 v3, s61\n" \
    "v_mov_b32 v10, s64\n" \
    "v_mov_b32 v11, s65\n" \
    "global_store_dwordx2 v3, v[10:11], %[ws] sc1\n" \
    "s_mov_b64 exec, -1\n" \
    "MN_NOF1_%=:\n" \
    "s_barrier\n" \
    "ds_read_b64_tr_b16 v[112:113], v20 offset:0\n" \
    "ds_read_b64_tr_b16 v[114:115], v20 offset:256\n" \
    "ds_read_b64_tr_b16 v[116:117], v20 offset:1024\n" \
    "ds_read_b64_tr_b16 v[118:119], v20 offset:1280\n" \
    "ds_read_b64_tr_b16 v[120:121], v20 offset:2048\n" \
    "ds_read_b64_tr_b16 v[122:123], v20 offset:2304\n" \
    "s_waitcnt lgkmcnt(4)\n" \
    "v_mfma_f32_32x32x16_bf16 v[64:79], v[112:115], v[128:131], 0\n" \
    "s_add_u32 s60, s60, 1\n" \
    "s_cmp_eq_u32 s60, 7\n" \
    "s_cselect_b32 s60, 0, s60\n" \
    "s_lshl_b32 s61, s60, 12\n" \
    "v_add_u32 v21, s61, v25\n" \
    "ds_read_b64_tr_b16 v[124:125], v20 offset:3072\n" \
    "ds_read_b64_tr_b16 v[126:127], v20 offset:3328\n" \
    "s_waitcnt lgkmcnt(4)\n" \
    "v_mfma_f32_32x32x16_bf16 v[64:79], v[116:119], v[132:135], v[64:79]\n" \
    "ds_read_b64_tr_b16 v[112:113], v21 offset:0\n" \
    "ds_read_b64_tr_b16 v[114:115], v21 offset:256\n" \
    "s_waitcnt lgkmcnt(4)\n" \
    "v_mfma_f32_32x32x16_bf16 v[64:79], v[120:123], v[136:139], v[64:79]\n" \
    "ds_read_b64_tr_b16 v[116:117], v21 offset:1024\n" \
    "ds_read_b64_tr_b16 v[118:119], v21 offset:1280\n" \
    "s_waitcnt lgkmcnt(4)\n" \
    "v_mfma_f32_32x32x16_bf16 v[64:79], v[124:127], v[140:143], v[64:79]\n" \
    "ds_read_b64_tr_b16 v[120:121], v21 offset:2048\n" \
    "ds_read_b64_tr_b16 v[122:123], v21 offset:2304\n" \
    "s_waitcnt lgkmcnt(4)\n" \
    "v_mfma_f32_32x32x16_bf16 v[64:79], v[112:115], v[144:147], v[64:79]\n" \
    "s_add_u32 s60, s60, 1\n" \
    "s_cmp_eq_u32 s60, 7\n" \
    "s_cselect_b32 s60, 0, s60\n" \
    "s_lshl_b32 s61, s60, 12\n" \
    "v_add_u32 v20, s61, v25\n" \
    "ds_read_b64_tr_b16 v[124:125], v21 offset:3072\n" \
    "ds_read_b64_tr_b16 v[126:127], v21 offset:3328\n" \
    "s_waitcnt lgkmcnt(4)\n" \
    "v_mfma_f32_32x32x16_bf16 v[64:79], v[116:119], v[148:151], v[64:79]\n" \
    "ds_read_b64_tr_b16 v[112:113], v20 offset:0\n" \
    "ds_read_b64_tr_b16 v[114:115], v20 offset:256\n" \
    "s_waitcnt lgkmcnt(4)\n" \
    "v_mfma_f32_32x32x16_bf16 v[64:79], v[120:123], v[152:155], v[64:79]\n" \
    "ds_read_b64_tr_b16 v[116:117], v20 offset:1024\n" \
    "ds_read_b64_tr_b16 v[118:119], v20 offset:1280\n" \
    "s_waitcnt lgkmcnt(4)\n" \
    "v_mfma_f32_32x32x16_bf16 v[64:79], v[124:127], v[156:159], v[64:79]\n" \
    "ds_read_b64_tr_b16 v[120:121], v20 offset:2048\n" \
    "ds_read_b64_tr_b16 v[122:123], v20 offset:2304\n" \
    "s_waitcnt lgkmcnt(4)\n" \
    "v_mfma_f32_32x32x16_bf16 v[64:79], v[112:115], v[160:163], v[64:79]\n" \
    "s_add_u32 s60, s60, 1\n" \
    "s_cmp_eq_u32 s60, 7\n" \
    "s_cselect_b32 s60, 0, s60\n" \
    "s_lshl_b32 s61, s60, 12\n" \
    "v_add_u32 v21, s61, v25\n" \
    "ds_read_b64_tr_b16 v[124:125], v20 offset:3072\n" \
    "ds_read_b64_tr_b16 v[126:127], v20 offset:3328\n" \
    "s_waitcnt lgkmcnt(4)\n" \
    "v_mfma_f32_32x32x16_bf16 v[64:79], v[116:119], v[164:167], v[64:79]\n" \
    "ds_read_b64_tr_b16 v[112:113], v21 offset:0\n" \
    "ds_read_b64_tr_b16 v[114:115], v21 offset:256\n" \
    "s_waitcnt lgkmcnt(4)\n" \
    "v_mfma_f32_32x32x16_bf16 v[64:79], v[120:123], v[168:171], v[64:79]\n" \
    "ds_read_b64_tr_b16 v[116:117], v21 offset:1024\n" \
    "ds_read_b64_tr_b16 v[118:119], v21 offset:1280\n" \
    "s_waitcnt lgkmcnt(4)\n" \
    "v_mfma_f32_32x32x16_bf16 v[64:79], v[124:127], v[172:175], v[64:79]\n" \
    "ds_read_b64_tr_b16 v[120:121], v21 offset:2048\n" \
    "ds_read_b64_tr_b16 v[122:123], v21 offset:2304\n" \
    "s_waitcnt lgkmcnt(4)\n" \
    "v_mfma_f32_32x32x16_bf16 v[64:79], v[112:115], v[176:179], v[64:79]\n" \
    "s_add_u32 s60, s60, 1\n" \
    "s_cmp_eq_u32 s60, 7\n" \
    "s_cselect_b32 s60, 0, s60\n" \
    "s_lshl_b32 s61, s60, 12\n" \
    "v_add_u32 v20, s61, v25\n" \
    "ds_read_b64_tr_b16 v[124:125], v21 offset:3072\n" \
    "ds_read_b64_tr_b16 v[126:127], v21 offset:3328\n" \
    "s_waitcnt lgkmcnt(4)\n" \
    "v_mfma_f32_32x32x16_bf16 v[64:79], v[116:119], v[180:183], v[64:79]\n" \
    "s_waitcnt lgkmcnt(2)\n" \
    "v_mfma_f32_32x32x16_bf16 v[64:79], v[120:123], v[184:187], v[64:79]\n" \
    "s_waitcnt lgkmcnt(0)\n" \
    "v_mfma_f32_32x32x16_bf16 v[64:79], v[124:127], v[188:191], v[64:79]\n" \
    "s_barrier\n" \
    "ds_read_b64_tr_b16 v[112:113], v20 offset:0\n" \
    "ds_read_b64_tr_b16 v[114:115], v20 offset:256\n" \
    "ds_read_b64_tr_b16 v[116:117], v20 offset:1024\n" \
    "ds_read_b64_tr_b16 v[118:119], v20 offset:1280\n" \
    "ds_read_b64_tr_b16 v[120:121], v20 offset:2048\n" \
    "ds_read_b64_tr_b16 v[122:123], v20 offset:2304\n" \
    "s_waitcnt lgkmcnt(4)\n" \
    "v_mfma_f32_32x32x16_bf16 v[64:79], v[112:115], v[192:195], v[64:79]\n" \
    "s_add_u32 s60, s60, 1\n" \
    "s_cmp_eq_u32 s60, 7\n" \
    "s_cselect_b32 s60, 0, s60\n" \
    "s_lshl_b32 s61, s60, 12\n" \
    "v_add_u32 v21, s61, v25\n" \
    "ds_read_b64_tr_b16 v[124:125], v20 offset:3072\n" \
    "ds_read_b64_tr_b16 v[126:127], v20 offset:3328\n" \
    "s_waitcnt lgkmcnt(4)\n" \
    "v_mfma_f32_32x32x16_bf16 v[64:79], v[116:119], v[196:199], v[64:79]\n" \
    "ds_read_b64_tr_b16 v[112:113], v21 offset:0\n" \
    "ds_read_b64_tr_b16 v[114:115], v21 offset:256\n" \
    "s_waitcnt lgkmcnt(4)\n" \
    "v_mfma_f32_32x32x16_bf16 v[64:79], v[120:123], v[200:203], v[64:79]\n" \
    "ds_read_b64_tr_b16 v[116:117], v21 offset:1024\n" \
    "ds_read_b64_tr_b16 v[118:119], v21 offset:1280\n" \
    "s_waitcnt lgkmcnt(4)\n" \
    "v_mfma_f32_32x32x16_bf16 v[64:79], v[124:127], v[204:207], v[64:79]\n" \
    "ds_read_b64_tr_b16 v[120:121], v21 offset:2048\n" \
    "ds_read_b64_tr_b16 v[122:123], v21 offset:2304\n" \
    "s_waitcnt lgkmcnt(4)\n" \
    "v_mfma_f32_32x32x16_bf16 v[64:79], v[112:115], v[208:211], v[64:79]\n" \
    "s_add_u32 s60, s60, 1\n" \
    "s_cmp_eq_u32 s60, 7\n" \
    "s_cselect_b32 s60, 0, s60\n" \
    "s_lshl_b32 s61, s60, 12\n" \
    "v_add_u32 v20, s61, v25\n" \
    "ds_read_b64_tr_b16 v[124:125], v21 offset:3072\n" \
    "ds_read_b64_tr_b16 v[126:127], v21 offset:3328\n" \
    "s_waitcnt lgkmcnt(4)\n" \
    "v_mfma_f32_32x32x16_bf16 v[64:79], v[116:119], v[212:215], v[64:79]\n" \
    "ds_read_b64_tr_b16 v[112:113], v20 offset:0\n" \
    "ds_read_b64_tr_b16 v[114:115], v20 offset:256\n" \
    "s_waitcnt lgkmcnt(4)\n" \
    "v_mfma_f32_32x32x16_bf16 v[64:79], v[120:123], v[216:219], v[64:79]\n" \
    "ds_read_b64_tr_b16 v[116:117], v20 offset:1024\n" \
    "ds_read_b64_tr_b16 v[118:119], v20 offset:1280\n" \
    "s_waitcnt lgkmcnt(4)\n" \
    "v_mfma_f32_32x32x16_bf16 v[64:79], v[124:127], v[220:223], v[64:79]\n" \
    "ds_read_b64_tr_b16 v[120:121], v20 offset:2048\n" \
    "ds_read_b64_tr_b16 v[122:123], v20 offset:2304\n" \
    "s_waitcnt lgkmcnt(4)\n" \
    "v_mfma_f32_32x32x16_bf16 v[64:79], v[112:115], v[224:227], v[64:79]\n" \
    "ds_read_b64_tr_b16 v[124:125], v20 offset:3072\n" \
    "ds_read_b64_tr_b16 v[126:127], v20 offset:3328\n" \
    "s_waitcnt lgkmcnt(4)\n" \
    "v_mfma_f32_32x32x16_bf16 v[64:79], v[116:119], v[228:231], v[64:79]\n" \
    "s_waitcnt lgkmcnt(2)\n" \
    "v_mfma_f32_32x32x16_bf16 v[64:79], v[120:123], v[232:235], v[64:79]\n" \
    "s_waitcnt lgkmcnt(0)\n" \
    "v_mfma_f32_32x32x16_bf16 v[64:79], v[124:127], v[236:239], v[64:79]\n" \
    "v_mov_b32 v10, 0x3e8b97f6\n" \
    "s_lshl_b32 s61, %[wv], 11\n" \
    "s_add_u32 s61, s61, 155136\n" \
    "v_add_u32 v3, s61, v1\n" \
    "s_nop 7\n" \
    "v_mul_f32 v8, 0xbfb8aa3b, v77\n" \
    "v_cvt_pk_bf16_f32 v9, v76, v76\n" \
    "v_exp_f32 v8, v8\n" \
    "v_pk_mul_f32 v[64:65], v[64:65], v[10:11] op_sel_hi:[1,0]\n" \
    "v_pk_mul_f32 v[66:67], v[66:67], v[10:11] op_sel_hi:[1,0]\n" \
    "v_pk_mul_f32 v[68:69], v[68:69], v[10:11] op_sel_hi:[1,0]\n" \
    "v_pk_mul_f32 v[70:71], v[70:71], v[10:11] op_sel_hi:[1,0]\n" \
    "v_pk_mul_f32 v[72:73], v[72:73], v[10:11] op_sel_hi:[1,0]\n" \
    "v_pk_mul_f32 v[74:75], v[74:75], v[10:11] op_sel_hi:[1,0]\n" \
    "v_pk_mul_f32 v[76:77], v[76:77], v[10:11] op_sel_hi:[1,0]\n" \
    "v_pk_mul_f32 v[78:79], v[78:79], v[10:11] op_sel_hi:[1,0]\n" \
    "v_add_f32 v8, 1.0, v8\n" \
    "v_cvt_pk_bf16_f32 v96, v64, v65\n" \
    "v_cvt_pk_bf16_f32 v97, v66, v67\n" \
    "v_cvt_pk_bf16_f32 v98, v68, v69\n" \
    "v_cvt_pk_bf16_f32 v99, v70, v71\n" \
    "v_cvt_pk_bf16_f32 v100, v72, v73\n" \
    "v_cvt_pk_bf16_f32 v101, v74, v75\n" \
    "v_cvt_pk_bf16_f32 v102, v76, v77\n" \
    "v_cvt_pk_bf16_f32 v103, v78, v79\n" \
    "v_rcp_f32 v8, v8\n" \
    "ds_write_b128 v3, v[96:99]\n" \
    "ds_write_b128 v3, v[100:103] offset:1024\n" \
    "s_cmp_lg_u32 %[idx], 14\n" \
    "s_cbranch_scc1 MN_NOKEY_%=\n" \
    "s_mov_b32 exec_lo, 0\n" \
    "v_and_b32 v9, 0xffff, v9\n" \
    "v_and_b32 v10, 0x7fff, v9\n" \
    "v_cmp_lt_u32 vcc, 0x7f80, v10\n" \
    "v_bfe_i32 v11, v9, 15, 1\n" \
    "v_and_b32 v11, 0x7fff, v11\n" \
    "v_or_b32 v11, 0x8000, v11\n" \
    "v_xor_b32 v9, v9, v11\n" \
    "v_cndmask_b32_e64 v9, v9, 0, vcc\n" \
    "s_lshl_b32 s61, %[t], 7\n" \
    "s_lshl_b32 s72, %[wv], 5\n" \
    "s_add_u32 s61, s61, s72\n" \
    "s_sub_u32 s61, 2079, s61\n" \
    "v_sub_u32 v10, s61, v0\n" \
    "v_lshl_or_b32 v9, v9, 11, v10\n" \
    "ds_write_b32 v26, v9\n" \
    "s_mov_b32 exec_lo, -1\n" \
    "s_waitcnt lgkmcnt(0)\n" \
    "s_barrier\n" \
    "MN_NOKEY_%=:\n" \
    "s_add_u32 s61, %[wv], 1\n" \
    "s_and_b32 s61, s61, 3\n" \
    "s_lshl_b32 s61, s61, 11\n" \
    "s_add_u32 s61, s61, 155136\n" \
    "v_add_u32 v20, s61, v1\n" \
    "s_add_u32 s61, %[wv], 2\n" \
    "s_and_b32 s61, s61, 3\n" \
    "s_lshl_b32 s61, s61, 11\n" \
    "s_add_u32 s61, s61, 155136\n" \
    "v_add_u32 v21, s61, v1\n" \
    "s_add_u32 s61, %[wv], 3\n" \
    "s_and_b32 s61, s61, 3\n" \
    "s_lshl_b32 s61, s61, 11\n" \
    "s_add_u32 s61, s61, 155136\n" \
    "v_add_u32 v22, s61, v1\n" \
    "s_add_u32 s61, %[wv], 4\n" \
    "s_and_b32 s61, s61, 3\n" \
    "s_lshl_b32 s61, s61, 11\n" \
    "s_add_u32 s61, s61, 155136\n" \
    "v_add_u32 v23, s61, v1\n" \
    "s_mov_b32 s70, 0\n" \
    "s_mov_b32 s68, 0\n" \
    "s_mov_b32 s76, 0\n" \
    "s_mov_b32 s77, -1\n" \
    "s_waitcnt lgkmcnt(0)\n" \
    "v_mov_b32 v184, 0\n" \
    "v_mov_b32 v185, 0\n" \
    "v_mov_b32 v186, 0\n" \
    "v_mov_b32 v187, 0\n" \
    "v_mov_b32 v188, 0\n" \
    "v_mov_b32 v189, 0\n" \
    "v_mov_b32 v190, 0\n" \
    "v_mov_b32 v191, 0\n" \
    "v_mov_b32 v232, 0\n" \
    "v_mov_b32 v233, 0\n" \
    "v_mov_b32 v234, 0\n" \
    "v_mov_b32 v235, 0\n" \
    "v_mov_b32 v236, 0\n" \
    "v_mov_b32 v237, 0\n" \
    "v_mov_b32 v238, 0\n" \
    "v_mov_b32 v239, 0\n" \
    "v_mov_b32 v240, 0xff800000\n" \
    "v_mov_b32 v241, 0xff800000\n" \
    "v_mov_b32 v242, 0xff800000\n" \
    "v_mov_b32 v243, 0xff800000\n" \
    "v_mfma_f32_32x32x16_bf16 v[64:79], v[184:187], v[184:187], 0\n" \
    "v_mfma_f32_32x32x16_bf16 v[80:95], v[184:187], v[184:187], 0\n" \
    "v_mfma_f32_32x32x16_bf16 v[96:111], v[184:187], v[184:187], 0\n" \
    "v_mfma_f32_32x32x16_bf16 v[112:127], v[184:187], v[184:187], 0\n" \
    "s_cmp_lt_u32 %[idx], 14\n" \
    "s_cbranch_scc1 MN_POLLD_%=\n" \
    "s_cmp_lg_u32 %[wv], 0\n" \
    "s_cbranch_scc1 MN_POLLD_%=\n" \
    "s_mov_b32 s63, 0x20000\n" \
    "MN_POLL_%=:\n" \
    "global_load_dwordx2 v[10:11], v27, %[ws] sc1\n" \
    "s_waitcnt vmcnt(0)\n" \
    "v_cmp_ne_u64 vcc, s[64:65], v[10:11]\n" \
    "s_sub_u32 s63, s63, 1\n" \
    "s_cbranch_vccz MN_POLLD_%=\n" \
    "s_cbranch_scc1 MN_POLLD_%=\n" \
    "s_sleep 1\n" \
    "s_branch MN_POLL_%=\n" \
    "MN_POLLD_%=:\n" \
    "s_barrier\n" \
    "buffer_load_dwordx2 v[128:129], v2, %[ratt], 0 offen offset:0 sc1\n" \
    "buffer_load_dwordx2 v[130:131], v2, %[ratt], 0 offen offset:512 sc1\n" \
    "buffer_load_dwordx2 v[132:133], v2, %[ratt], 0 offen offset:1024 sc1\n" \
    "buffer_load_dwordx2 v[134:135], v2, %[ratt], 0 offen offset:1536 sc1\n" \
    "buffer_load_dwordx4 v[136:139], v1, %[ratt], 0 offen offset:2048 sc1\n" \
    "buffer_load_dwordx4 v[140:143], v1, %[ratt], 0 offen offset:3072 sc1\n" \
    "s_mov_b32 s66, 0x1000\n" \
    "ds_read_b128 v[32:35], v20\n" \
    "ds_read_b128 v[36:39], v20 offset:1024\n" \
    "ds_read_b128 v[40:43], v21\n" \
    "ds_read_b128 v[44:47], v21 offset:1024\n" \
    "ds_read_b128 v[48:51], v22\n" \
    "ds_read_b128 v[52:55], v22 offset:1024\n" \
    "ds_read_b128 v[56:59], v23\n" \
    "ds_read_b128 v[60:63], v23 offset:1024\n" \
    "s_waitcnt lgkmcnt(0)\n" \
    "MN_ATT_%=:\n" \
    "v_cndmask_b32_e64 v38, v38, 0, s[76:77]\n" \
    "v_cndmask_b32_e64 v39, v39, 0, s[76:77]\n" \
    "v_cndmask_b32_e64 v46, v46, 0, s[76:77]\n" \
    "v_cndmask_b32_e64 v47, v47, 0, s[76:77]\n" \
    "v_cndmask_b32_e64 v54, v54, 0, s[76:77]\n" \
    "v_cndmask_b32_e64 v55, v55, 0, s[76:77]\n" \
    "v_cndmask_b32_e64 v62, v62, 0, s[76:77]\n" \
    "v_cndmask_b32_e64 v63, v63, 0, s[76:77]\n" \
    "s_waitcnt vmcnt(2)\n" \
    "MN_MAXL_%=:\n" \
    "v_mfma_f32_32x32x16_bf16 v[192:207], v[128:131], v[32:35], 0\n" \
    "v_mfma_f32_32x32x16_bf16 v[192:207], v[132:135], v[36:39], v[192:207]\n" \
    "v_mfma_f32_32x32x16_bf16 v[208:223], v[128:131], v[40:43], 0\n" \
    "buffer_load_dwordx2 v[144:145], v2, %[ratt], s66 offen offset:0 sc1\n" \
    "buffer_load_dwordx2 v[146:147], v2, %[ratt], s66 offen offset:512 sc1\n" \
    "buffer_load_dwordx2 v[148:149], v2, %[ratt], s66 offen offset:1024 sc1\n" \
    "buffer_load_dwordx2 v[150:151], v2, %[ratt], s66 offen offset:1536 sc1\n" \
    "buffer_load_dwordx4 v[152:155], v1, %[ratt], s66 offen offset:2048 sc1\n" \
    "buffer_load_dwordx4 v[156:159], v1, %[ratt], s66 offen offset:3072 sc1\n" \
    "v_mfma_f32_32x32x16_bf16 v[208:223], v[132:135], v[44:47], v[208:223]\n" \
    "s_nop 3\n" \
    "v_max3_f32 v240, v240, v192, v193\n" \
    "v_max3_f32 v244, v194, v194, v195\n" \
    "v_mfma_f32_32x32x16_bf16 v[12:27], v[128:131], v[48:51], 0\n" \
    "v_max3_f32 v240, v240, v196, v197\n" \
    "v_max3_f32 v244, v244, v198, v199\n" \
    "v_max3_f32 v240, v240, v200, v201\n" \
    "v_max3_f32 v244, v244, v202, v203\n" \
    "v_max3_f32 v240, v240, v204, v205\n" \
    "v_max3_f32 v244, v244, v206, v207\n" \
    "v_mfma_f32_32x32x16_bf16 v[12:27], v[132:135], v[52:55], v[12:27]\n" \
    "v_max3_f32 v241, v241, v208, v209\n" \
    "v_max3_f32 v245, v210, v210, v211\n" \
    "v_max3_f32 v241, v241, v212, v213\n" \
    "v_max3_f32 v245, v245, v214, v215\n" \
    "v_max3_f32 v241, v241, v216, v217\n" \
    "v_max3_f32 v245, v245, v218, v219\n" \
    "v_mfma_f32_32x32x16_bf16 v[192:207], v[128:131], v[56:59], 0\n" \
    "v_max3_f32 v241, v241, v220, v221\n" \
    "v_max3_f32 v245, v245, v222, v223\n" \
    "v_max_f32 v240, v240, v244\n" \
    "v_max_f32 v241, v241, v245\n" \
    "v_mfma_f32_32x32x16_bf16 v[192:207], v[132:135], v[60:63], v[192:207]\n" \
    "v_mov_b32 v244, v240\n" \
    "v_mov_b32 v245, v241\n" \
    "v_max3_f32 v242, v242, v12, v13\n" \
    "v_max3_f32 v246, v14, v14, v15\n" \
    "v_max3_f32 v242, v242, v16, v17\n" \
    "v_max3_f32 v246, v246, v18, v19\n" \
    "v_max3_f32 v242, v242, v20, v21\n" \
    "v_max3_f32 v246, v246, v22, v23\n" \
    "v_max3_f32 v242, v242, v24, v25\n" \
    "v_max3_f32 v246, v246, v26, v27\n" \
    "v_permlane32_swap_b32 v240, v244\n" \
    "v_permlane32_swap_b32 v241, v245\n" \
    "v_max_f32 v242, v242, v246\n" \
    "v_max_f32 v240, v240, v244\n" \
    "v_mov_b32 v246, v242\n" \
    "v_max_f32 v241, v241, v245\n" \
    "v_max3_f32 v243, v243, v192, v193\n" \
    "v_max3_f32 v247, v194, v194, v195\n" \
    "v_max3_f32 v243, v243, v196, v197\n" \
    "v_max3_f32 v247, v247, v198, v199\n" \
    "v_max3_f32 v243, v243, v200, v201\n" \
    "v_max3_f32 v247, v247, v202, v203\n" \
    "v_max3_f32 v243, v243, v204, v205\n" \
    "v_max3_f32 v247, v247, v206, v207\n" \
    "v_permlane32_swap_b32 v242, v246\n" \
    "v_max_f32 v243, v243, v247\n" \
    "v_max_f32 v242, v242, v246\n" \
    "v_mov_b32 v247, v243\n" \
    "s_cmp_lg_u32 s70, 0\n" \
    "s_cbranch_scc1 MN_MAXT_%=\n" \
    "v_permlane32_swap_b32 v243, v247\n" \
    "v_cvt_pk_bf16_f32 v244, -v240, 0\n" \
    "v_cvt_pk_bf16_f32 v245, -v241, 0\n" \
    "v_cvt_pk_bf16_f32 v246, -v242, 0\n" \
    "v_max_f32 v243, v243, v247\n" \
    "v_cndmask_b32_e64 v38, v38, v244, s[76:77]\n" \
    "v_cndmask_b32_e64 v46, v46, v245, s[76:77]\n" \
    "v_cndmask_b32_e64 v54, v54, v246, s[76:77]\n" \
    "v_cvt_pk_bf16_f32 v247, -v243, 0\n" \
    "v_mfma_f32_32x32x16_bf16 v[192:207], v[128:131], v[32:35], 0\n" \
    "s_add_u32 s66, s66, 0x1000\n" \
    "buffer_load_dwordx2 v[160:161], v2, %[ratt], s66 offen offset:0 sc1\n" \
    "buffer_load_dwordx2 v[162:163], v2, %[ratt], s66 offen offset:512 sc1\n" \
    "buffer_load_dwordx2 v[164:165], v2, %[ratt], s66 offen offset:1024 sc1\n" \
    "buffer_load_dwordx2 v[166:167], v2, %[ratt], s66 offen offset:1536 sc1\n" \
    "buffer_load_dwordx4 v[168:171], v1, %[ratt], s66 offen offset:2048 sc1\n" \
    "buffer_load_dwordx4 v[172:175], v1, %[ratt], s66 offen offset:3072 sc1\n" \
    "v_cndmask_b32_e64 v62, v62, v247, s[76:77]\n" \
    "v_mfma_f32_32x32x16_bf16 v[192:207], v[132:135], v[36:39], v[192:207]\n" \
    "v_lshlrev_b32 v240, 16, v244\n" \
    "v_lshlrev_b32 v241, 16, v245\n" \
    "v_lshlrev_b32 v242, 16, v246\n" \
    "v_lshlrev_b32 v243, 16, v247\n" \
    "v_xor_b32 v240, 0x80000000, v240\n" \
    "v_xor_b32 v241, 0x80000000, v241\n" \
    "v_xor_b32 v242, 0x80000000, v242\n" \
    "v_xor_b32 v243, 0x80000000, v243\n" \
    "MN_GO_%=:\n" \
    "v_mov_b32 v222, 0\n" \
    "v_mov_b32 v223, 0\n" \
    "s_add_u32 s66, s66, 0x1000\n" \
    "s_mov_b32 s69, 4\n" \
    "s_waitcnt vmcnt(12)\n" \
    "MN_LOOP_%=:\n" \
    "v_mfma_f32_32x32x16_bf16 v[112:127], v[184:187], v[232:235], v[112:127]\n" \
    "v_cvt_pk_bf16_f32 v239, v222, v223\n" \
    "v_exp_f32 v192, v192\n" \
    "v_exp_f32 v193, v193\n" \
    "v_exp_f32 v194, v194\n" \
    "v_cvt_pk_bf16_f32 v224, v192, v193\n" \
    "v_exp_f32 v195, v195\n" \
    "v_mfma_f32_32x32x16_bf16 v[208:223], v[128:131], v[40:43], 0\n" \
    "v_cvt_pk_bf16_f32 v225, v194, v195\n" \
    "v_exp_f32 v196, v196\n" \
    "v_exp_f32 v197, v197\n" \
    "v_exp_f32 v198, v198\n" \
    "v_cvt_pk_bf16_f32 v226, v196, v197\n" \
    "v_exp_f32 v199, v199\n" \
    "v_mfma_f32_32x32x16_bf16 v[208:223], v[132:135], v[44:47], v[208:223]\n" \
    "v_cvt_pk_bf16_f32 v227, v198, v199\n" \
    "v_exp_f32 v200, v200\n" \
    "v_exp_f32 v201, v201\n" \
    "v_exp_f32 v202, v202\n" \
    "v_cvt_pk_bf16_f32 v228, v200, v201\n" \
    "v_exp_f32 v203, v203\n" \
    "v_mfma_f32_32x32x16_bf16 v[112:127], v[188:191], v[236:239], v[112:127]\n" \
    "v_cvt_pk_bf16_f32 v229, v202, v203\n" \
    "v_exp_f32 v204, v204\n" \
    "v_exp_f32 v205, v205\n" \
    "v_exp_f32 v206, v206\n" \
    "v_cvt_pk_bf16_f32 v230, v204, v205\n" \
    "v_exp_f32 v207, v207\n" \
    "v_mfma_f32_32x32x16_bf16 v[64:79], v[136:139], v[224:227], v[64:79]\n" \
    "buffer_load_dwordx2 v[176:177], v2, %[ratt], s66 offen offset:0 sc1\n" \
    "buffer_load_dwordx2 v[178:179], v2, %[ratt], s66 offen offset:512 sc1\n" \
    "buffer_load_dwordx2 v[180:181], v2, %[ratt], s66 offen offset:1024 sc1\n" \
    "buffer_load_dwordx2 v[182:183], v2, %[ratt], s66 offen offset:1536 sc1\n" \
    "buffer_load_dwordx4 v[184:187], v1, %[ratt], s66 offen offset:2048 sc1\n" \
    "buffer_load_dwordx4 v[188:191], v1, %[ratt], s66 offen offset:3072 sc1\n" \
    "v_cvt_pk_bf16_f32 v231, v206, v207\n" \
    "v_exp_f32 v208, v208\n" \
    "v_exp_f32 v209, v209\n" \
    "v_exp_f32 v210, v210\n" \
    "v_cvt_pk_bf16_f32 v232, v208, v209\n" \
    "v_exp_f32 v211, v211\n" \
    "v_mfma_f32_32x32x16_bf16 v[192:207], v[128:131], v[48:51], 0\n" \
    "s_add_u32 s66, s66, 0x1000\n" \
    "v_cvt_pk_bf16_f32 v233, v210, v211\n" \
    "v_exp_f32 v212, v212\n" \
    "v_exp_f32 v213, v213\n" \
    "v_exp_f32 v214, v214\n" \
    "v_cvt_pk_bf16_f32 v234, v212, v213\n" \
    "v_exp_f32 v215, v215\n" \
    "v_mfma_f32_32x32x16_bf16 v[192:207], v[132:135], v[52:55], v[192:207]\n" \
    "v_cvt_pk_bf16_f32 v235, v214, v215\n" \
    "v_exp_f32 v216, v216\n" \
    "v_exp_f32 v217, v217\n" \
    "v_exp_f32 v218, v218\n" \
    "v_cvt_pk_bf16_f32 v236, v216, v217\n" \
    "v_exp_f32 v219, v219\n" \
    "v_mfma_f32_32x32x16_bf16 v[64:79], v[140:143], v[228:231], v[64:79]\n" \
    "v_cvt_pk_bf16_f32 v237, v218, v219\n" \
    "v_exp_f32 v220, v220\n" \
    "v_exp_f32 v221, v221\n" \
    "v_exp_f32 v222, v222\n" \
    "v_cvt_pk_bf16_f32 v238, v220, v221\n" \
    "v_exp_f32 v223, v223\n" \
    "v_mfma_f32_32x32x16_bf16 v[80:95], v[136:139], v[232:235], v[80:95]\n" \
    "v_cvt_pk_bf16_f32 v239, v222, v223\n" \
    "v_exp_f32 v192, v192\n" \
    "v_exp_f32 v193, v193\n" \
    "v_exp_f32 v194, v194\n" \
    "v_cvt_pk_bf16_f32 v224, v192, v193\n" \
    "v_exp_f32 v195, v195\n" \
    "v_mfma_f32_32x32x16_bf16 v[208:223], v[128:131], v[56:59], 0\n" \
    "v_cvt_pk_bf16_f32 v225, v194, v195\n" \
    "v_exp_f32 v196, v196\n" \
    "v_exp_f32 v197, v197\n" \
    "v_exp_f32 v198, v198\n" \
    "v_cvt_pk_bf16_f32 v226, v196, v197\n" \
    "v_exp_f32 v199, v199\n" \
    "v_mfma_f32_32x32x16_bf16 v[208:223], v[132:135], v[60:63], v[208:223]\n" \
    "v_cvt_pk_bf16_f32 v227, v198, v199\n" \
    "v_exp_f32 v200, v200\n" \
    "v_exp_f32 v201, v201\n" \
    "v_exp_f32 v202, v202\n" \
    "v_cvt_pk_bf16_f32 v228, v200, v201\n" \
    "v_exp_f32 v203, v203\n" \
    "v_mfma_f32_32x32x16_bf16 v[80:95], v[140:143], v[236:239], v[80:95]\n" \
    "v_cvt_pk_bf16_f32 v229, v202, v203\n" \
    "v_exp_f32 v204, v204\n" \
    "v_exp_f32 v205, v205\n" \
    "v_exp_f32 v206, v206\n" \
    "v_cvt_pk_bf16_f32 v230, v204, v205\n" \
    "v_exp_f32 v207, v207\n" \
    "v_mfma_f32_32x32x16_bf16 v[96:111], v[136:139], v[224:227], v[96:111]\n" \
    "s_waitcnt vmcnt(12)\n" \
    "v_cvt_pk_bf16_f32 v231, v206, v207\n" \
    "v_exp_f32 v208, v208\n" \
    "v_exp_f32 v209, v209\n" \
    "v_exp_f32 v210, v210\n" \
    "v_cvt_pk_bf16_f32 v232, v208, v209\n" \
    "v_exp_f32 v211, v211\n" \
    "v_mfma_f32_32x32x16_bf16 v[192:207], v[144:147], v[32:35], 0\n" \
    "v_cvt_pk_bf16_f32 v233, v210, v211\n" \
    "v_exp_f32 v212, v212\n" \
    "v_exp_f32 v213, v213\n" \
    "v_exp_f32 v214, v214\n" \
    "v_cvt_pk_bf16_f32 v234, v212, v213\n" \
    "v_exp_f32 v215, v215\n" \
    "v_mfma_f32_32x32x16_bf16 v[192:207], v[148:151], v[36:39], v[192:207]\n" \
    "v_cvt_pk_bf16_f32 v235, v214, v215\n" \
    "v_exp_f32 v216, v216\n" \
    "v_exp_f32 v217, v217\n" \
    "v_exp_f32 v218, v218\n" \
    "v_cvt_pk_bf16_f32 v236, v216, v217\n" \
    "v_exp_f32 v219, v219\n" \
    "v_mfma_f32_32x32x16_bf16 v[96:111], v[140:143], v[228:231], v[96:111]\n" \
    "v_cvt_pk_bf16_f32 v237, v218, v219\n" \
    "v_exp_f32 v220, v220\n" \
    "v_exp_f32 v221, v221\n" \
    "v_exp_f32 v222, v222\n" \
    "v_cvt_pk_bf16_f32 v238, v220, v221\n" \
    "v_exp_f32 v223, v223\n" \
    "v_mfma_f32_32x32x16_bf16 v[112:127], v[136:139], v[232:235], v[112:127]\n" \
    "v_cvt_pk_bf16_f32 v239, v222, v223\n" \
    "v_exp_f32 v192, v192\n" \
    "v_exp_f32 v193, v193\n" \
    "v_exp_f32 v194, v194\n" \
    "v_cvt_pk_bf16_f32 v224, v192, v193\n" \
    "v_exp_f32 v195, v195\n" \
    "v_mfma_f32_32x32x16_bf16 v[208:223], v[144:147], v[40:43], 0\n" \
    "v_cvt_pk_bf16_f32 v225, v194, v195\n" \
    "v_exp_f32 v196, v196\n" \
    "v_exp_f32 v197, v197\n" \
    "v_exp_f32 v198, v198\n" \
    "v_cvt_pk_bf16_f32 v226, v196, v197\n" \
    "v_exp_f32 v199, v199\n" \
    "v_mfma_f32_32x32x16_bf16 v[208:223], v[148:151], v[44:47], v[208:223]\n" \
    "v_cvt_pk_bf16_f32 v227, v198, v199\n" \
    "v_exp_f32 v200, v200\n" \
    "v_exp_f32 v201, v201\n" \
    "v_exp_f32 v202, v202\n" \
    "v_cvt_pk_bf16_f32 v228, v200, v201\n" \
    "v_exp_f32 v203, v203\n" \
    "v_mfma_f32_32x32x16_bf16 v[112:127], v[140:143], v[236:239], v[112:127]\n" \
    "v_cvt_pk_bf16_f32 v229, v202, v203\n" \
    "v_exp_f32 v204, v204\n" \
    "v_exp_f32 v205, v205\n" \
    "v_exp_f32 v206, v206\n" \
    "v_cvt_pk_bf16_f32 v230, v204, v205\n" \
    "v_exp_f32 v207, v207\n" \
    "v_mfma_f32_32x32x16_bf16 v[64:79], v[152:155], v[224:227], v[64:79]\n" \
    "buffer_load_dwordx2 v[128:129], v2, %[ratt], s66 offen offset:0 sc1\n" \
    "buffer_load_dwordx2 v[130:131], v2, %[ratt], s66 offen offset:512 sc1\n" \
    "buffer_load_dwordx2 v[132:133], v2, %[ratt], s66 offen offset:1024 sc1\n" \
    "buffer_load_dwordx2 v[134:135], v2, %[ratt], s66 offen offset:1536 sc1\n" \
    "buffer_load_dwordx4 v[136:139], v1, %[ratt], s66 offen offset:2048 sc1\n" \
    "buffer_load_dwordx4 v[140:143], v1, %[ratt], s66 offen offset:3072 sc1\n" \
    "v_cvt_pk_bf16_f32 v231, v206, v207\n" \
    "v_exp_f32 v208, v208\n" \
    "v_exp_f32 v209, v209\n" \
    "v_exp_f32 v210, v210\n" \
    "v_cvt_pk_bf16_f32 v232, v208, v209\n" \
    "v_exp_f32 v211, v211\n" \
    "v_mfma_f32_32x32x16_bf16 v[192:207], v[144:147], v[48:51], 0\n" \
    "s_add_u32 s66, s66, 0x1000\n" \
    "v_cvt_pk_bf16_f32 v233, v210, v211\n" \
    "v_exp_f32 v212, v212\n" \
    "v_exp_f32 v213, v213\n" \
    "v_exp_f32 v214, v214\n" \
    "v_cvt_pk_bf16_f32 v234, v212, v213\n" \
    "v_exp_f32 v215, v215\n" \
    "v_mfma_f32_32x32x16_bf16 v[192:207], v[148:151], v[52:55], v[192:207]\n" \
    "v_cvt_pk_bf16_f32 v235, v214, v215\n" \
    "v_exp_f32 v216, v216\n" \
    "v_exp_f32 v217, v217\n" \
    "v_exp_f32 v218, v218\n" \
    "v_cvt_pk_bf16_f32 v236, v216, v217\n" \
    "v_exp_f32 v219, v219\n" \
    "v_mfma_f32_32x32x16_bf16 v[64:79], v[156:159], v[228:231], v[64:79]\n" \
    "v_cvt_pk_bf16_f32 v237, v218, v219\n" \
    "v_exp_f32 v220, v220\n" \
    "v_exp_f32 v221, v221\n" \
    "v_exp_f32 v222, v222\n" \
    "v_cvt_pk_bf16_f32 v238, v220, v221\n" \
    "v_exp_f32 v223, v223\n" \
    "v_mfma_f32_32x32x16_bf16 v[80:95], v[152:155], v[232:235], v[80:95]\n" \
    "v_cvt_pk_bf16_f32 v239, v222, v223\n" \
    "v_exp_f32 v192, v192\n" \
    "v_exp_f32 v193, v193\n" \
    "v_exp_f32 v194, v194\n" \
    "v_cvt_pk_bf16_f32 v224, v192, v193\n" \
    "v_exp_f32 v195, v195\n" \
    "v_mfma_f32_32x32x16_bf16 v[208:223], v[144:147], v[56:59], 0\n" \
    "v_cvt_pk_bf16_f32 v225, v194, v195\n" \
    "v_exp_f32 v196, v196\n" \
    "v_exp_f32 v197, v197\n" \
    "v_exp_f32 v198, v198\n" \
    "v_cvt_pk_bf16_f32 v226, v196, v197\n" \
    "v_exp_f32 v199, v199\n" \
    "v_mfma_f32_32x32x16_bf16 v[208:223], v[148:151], v[60:63], v[208:223]\n" \
    "v_cvt_pk_bf16_f32 v227, v198, v199\n" \
    "v_exp_f32 v200, v200\n" \
    "v_exp_f32 v201, v201\n" \
    "v_exp_f32 v202, v202\n" \
    "v_cvt_pk_bf16_f32 v228, v200, v201\n" \
    "v_exp_f32 v203, v203\n" \
    "v_mfma_f32_32x32x16_bf16 v[80:95], v[156:159], v[236:239], v[80:95]\n" \
    "v_cvt_pk_bf16_f32 v229, v202, v203\n" \
    "v_exp_f32 v204, v204\n" \
    "v_exp_f32 v205, v205\n" \
    "v_exp_f32 v206, v206\n" \
    "v_cvt_pk_bf16_f32 v230, v204, v205\n" \
    "v_exp_f32 v207, v207\n" \
    "v_mfma_f32_32x32x16_bf16 v[96:111], v[152:155], v[224:227], v[96:111]\n" \
    "s_waitcnt vmcnt(12)\n" \
    "v_cvt_pk_bf16_f32 v231, v206, v207\n" \
    "v_exp_f32 v208, v208\n" \
    "v_exp_f32 v209, v209\n" \
    "v_exp_f32 v210, v210\n" \
    "v_cvt_pk_bf16_f32 v232, v208, v209\n" \
    "v_exp_f32 v211, v211\n" \
    "v_mfma_f32_32x32x16_bf16 v[192:207], v[160:163], v[32:35], 0\n" \
    "v_cvt_pk_bf16_f32 v233, v210, v211\n" \
    "v_exp_f32 v212, v212\n" \
    "v_exp_f32 v213, v213\n" \
    "v_exp_f32 v214, v214\n" \
    "v_cvt_pk_bf16_f32 v234, v212, v213\n" \
    "v_exp_f32 v215, v215\n" \
    "v_mfma_f32_32x32x16_bf16 v[192:207], v[164:167], v[36:39], v[192:207]\n" \
    "v_cvt_pk_bf16_f32 v235, v214, v215\n" \
    "v_exp_f32 v216, v216\n" \
    "v_exp_f32 v217, v217\n" \
    "v_exp_f32 v218, v218\n" \
    "v_cvt_pk_bf16_f32 v236, v216, v217\n" \
    "v_exp_f32 v219, v219\n" \
    "v_mfma_f32_32x32x16_bf16 v[96:111], v[156:159], v[228:231], v[96:111]\n" \
    "v_cvt_pk_bf16_f32 v237, v218, v219\n" \
    "v_exp_f32 v220, v220\n" \
    "v_exp_f32 v221, v221\n" \
    "v_exp_f32 v222, v222\n" \
    "v_cvt_pk_bf16_f32 v238, v220, v221\n" \
    "v_exp_f32 v223, v223\n" \
    "v_mfma_f32_32x32x16_bf16 v[112:127], v[152:155], v[232:235], v[112:127]\n" \
    "v_cvt_pk_bf16_f32 v239, v222, v223\n" \
    "v_exp_f32 v192, v192\n" \
    "v_exp_f32 v193, v193\n" \
    "v_exp_f32 v194, v194\n" \
    "v_cvt_pk_bf16_f32 v224, v192, v193\n" \
    "v_exp_f32 v195, v195\n" \
    "v_mfma_f32_32x32x16_bf16 v[208:223], v[160:163], v[40:43], 0\n" \
    "v_cvt_pk_bf16_f32 v225, v194, v195\n" \
    "v_exp_f32 v196, v196\n" \
    "v_exp_f32 v197, v197\n" \
    "v_exp_f32 v198, v198\n" \
    "v_cvt_pk_bf16_f32 v226, v196, v197\n" \
    "v_exp_f32 v199, v199\n" \
    "v_mfma_f32_32x32x16_bf16 v[208:223], v[164:167], v[44:47], v[208:223]\n" \
    "v_cvt_pk_bf16_f32 v227, v198, v199\n" \
    "v_exp_f32 v200, v200\n" \
    "v_exp_f32 v201, v201\n" \
    "v_exp_f32 v202, v202\n" \
    "v_cvt_pk_bf16_f32 v228, v200, v201\n" \
    "v_exp_f32 v203, v203\n" \
    "v_mfma_f32_32x32x16_bf16 v[112:127], v[156:159], v[236:239], v[112:127]\n" \
    "v_cvt_pk_bf16_f32 v229, v202, v203\n" \
    "v_exp_f32 v204, v204\n" \
    "v_exp_f32 v205, v205\n" \
    "v_exp_f32 v206, v206\n" \
    "v_cvt_pk_bf16_f32 v230, v204, v205\n" \
    "v_exp_f32 v207, v207\n" \
    "v_mfma_f32_32x32x16_bf16 v[64:79], v[168:171], v[224:227], v[64:79]\n" \
    "buffer_load_dwordx2 v[144:145], v2, %[ratt], s66 offen offset:0 sc1\n" \
    "buffer_load_dwordx2 v[146:147], v2, %[ratt], s66 offen offset:512 sc1\n" \
    "buffer_load_dwordx2 v[148:149], v2, %[ratt], s66 offen offset:1024 sc1\n" \
    "buffer_load_dwordx2 v[150:151], v2, %[ratt], s66 offen offset:1536 sc1\n" \
    "buffer_load_dwordx4 v[152:155], v1, %[ratt], s66 offen offset:2048 sc1\n" \
    "buffer_load_dwordx4 v[156:159], v1, %[ratt], s66 offen offset:3072 sc1\n" \
    "v_cvt_pk_bf16_f32 v231, v206, v207\n" \
    "v_exp_f32 v208, v208\n" \
    "v_exp_f32 v209, v209\n" \
    "v_exp_f32 v210, v210\n" \
    "v_cvt_pk_bf16_f32 v232, v208, v209\n" \
    "v_exp_f32 v211, v211\n" \
    "v_mfma_f32_32x32x16_bf16 v[192:207], v[160:163], v[48:51], 0\n" \
    "s_add_u32 s66, s66, 0x1000\n" \
    "v_cvt_pk_bf16_f32 v233, v210, v211\n" \
    "v_exp_f32 v212, v212\n" \
    "v_exp_f32 v213, v213\n" \
    "v_exp_f32 v214, v214\n" \
    "v_cvt_pk_bf16_f32 v234, v212, v213\n" \
    "v_exp_f32 v215, v215\n" \
    "v_mfma_f32_32x32x16_bf16 v[192:207], v[164:167], v[52:55], v[192:207]\n" \
    "v_cvt_pk_bf16_f32 v235, v214, v215\n" \
    "v_exp_f32 v216, v216\n" \
    "v_exp_f32 v217, v217\n" \
    "v_exp_f32 v218, v218\n" \
    "v_cvt_pk_bf16_f32 v236, v216, v217\n" \
    "v_exp_f32 v219, v219\n" \
    "v_mfma_f32_32x32x16_bf16 v[64:79], v[172:175], v[228:231], v[64:79]\n" \
    "v_cvt_pk_bf16_f32 v237, v218, v219\n" \
    "v_exp_f32 v220, v220\n" \
    "v_exp_f32 v221, v221\n" \
    "v_exp_f32 v222, v222\n" \
    "v_cvt_pk_bf16_f32 v238, v220, v221\n" \
    "v_exp_f32 v223, v223\n" \
    "v_mfma_f32_32x32x16_bf16 v[80:95], v[168:171], v[232:235], v[80:95]\n" \
    "v_cvt_pk_bf16_f32 v239, v222, v223\n" \
    "v_exp_f32 v192, v192\n" \
    "v_exp_f32 v193, v193\n" \
    "v_exp_f32 v194, v194\n" \
    "v_cvt_pk_bf16_f32 v224, v192, v193\n" \
    "v_exp_f32 v195, v195\n" \
    "v_mfma_f32_32x32x16_bf16 v[208:223], v[160:163], v[56:59], 0\n" \
    "v_cvt_pk_bf16_f32 v225, v194, v195\n" \
    "v_exp_f32 v196, v196\n" \
    "v_exp_f32 v197, v197\n" \
    "v_exp_f32 v198, v198\n" \
    "v_cvt_pk_bf16_f32 v226, v196, v197\n" \
    "v_exp_f32 v199, v199\n" \
    "v_mfma_f32_32x32x16_bf16 v[208:223], v[164:167], v[60:63], v[208:223]\n" \
    "v_cvt_pk_bf16_f32 v227, v198, v199\n" \
    "v_exp_f32 v200, v200\n" \
    "v_exp_f32 v201, v201\n" \
    "v_exp_f32 v202, v202\n" \
    "v_cvt_pk_bf16_f32 v228, v200, v201\n" \
    "v_exp_f32 v203, v203\n" \
    "v_mfma_f32_32x32x16_bf16 v[80:95], v[172:175], v[236:239], v[80:95]\n" \
    "v_cvt_pk_bf16_f32 v229, v202, v203\n" \
    "v_exp_f32 v204, v204\n" \
    "v_exp_f32 v205, v205\n" \
    "v_exp_f32 v206, v206\n" \
    "v_cvt_pk_bf16_f32 v230, v204, v205\n" \
    "v_exp_f32 v207, v207\n" \
    "v_mfma_f32_32x32x16_bf16 v[96:111], v[168:171], v[224:227], v[96:111]\n" \
    "s_waitcnt vmcnt(12)\n" \
    "v_cvt_pk_bf16_f32 v231, v206, v207\n" \
    "v_exp_f32 v208, v208\n" \
    "v_exp_f32 v209, v209\n" \
    "v_exp_f32 v210, v210\n" \
    "v_cvt_pk_bf16_f32 v232, v208, v209\n" \
    "v_exp_f32 v211, v211\n" \
    "v_mfma_f32_32x32x16_bf16 v[192:207], v[176:179], v[32:35], 0\n" \
    "v_cvt_pk_bf16_f32 v233, v210, v211\n" \
    "v_exp_f32 v212, v212\n" \
    "v_exp_f32 v213, v213\n" \
    "v_exp_f32 v214, v214\n" \
    "v_cvt_pk_bf16_f32 v234, v212, v213\n" \
    "v_exp_f32 v215, v215\n" \
    "v_mfma_f32_32x32x16_bf16 v[192:207], v[180:183], v[36:39], v[192:207]\n" \
    "s_sub_u32 s69, s69, 1\n" \
    "v_cvt_pk_bf16_f32 v235, v214, v215\n" \
    "v_exp_f32 v216, v216\n" \
    "v_exp_f32 v217, v217\n" \
    "v_exp_f32 v218, v218\n" \
    "v_cvt_pk_bf16_f32 v236, v216, v217\n" \
    "v_exp_f32 v219, v219\n" \
    "v_mfma_f32_32x32x16_bf16 v[96:111], v[172:175], v[228:231], v[96:111]\n" \
    "s_cmp_eq_u32 s69, 0\n" \
    "v_cvt_pk_bf16_f32 v237, v218, v219\n" \
    "v_exp_f32 v220, v220\n" \
    "v_exp_f32 v221, v221\n" \
    "v_exp_f32 v222, v222\n" \
    "v_cvt_pk_bf16_f32 v238, v220, v221\n" \
    "v_exp_f32 v223, v223\n" \
    "s_cbranch_scc1 MN_PEEL_%=\n" \
    "v_mfma_f32_32x32x16_bf16 v[112:127], v[168:171], v[232:235], v[112:127]\n" \
    "v_cvt_pk_bf16_f32 v239, v222, v223\n" \
    "v_exp_f32 v192, v192\n" \
    "v_exp_f32 v193, v193\n" \
    "v_exp_f32 v194, v194\n" \
    "v_cvt_pk_bf16_f32 v224, v192, v193\n" \
    "v_exp_f32 v195, v195\n" \
    "v_mfma_f32_32x32x16_bf16 v[208:223], v[176:179], v[40:43], 0\n" \
    "v_cvt_pk_bf16_f32 v225, v194, v195\n" \
    "v_exp_f32 v196, v196\n" \
    "v_exp_f32 v197, v197\n" \
    "v_exp_f32 v198, v198\n" \
    "v_cvt_pk_bf16_f32 v226, v196, v197\n" \
    "v_exp_f32 v199, v199\n" \
    "v_mfma_f32_32x32x16_bf16 v[208:223], v[180:183], v[44:47], v[208:223]\n" \
    "v_cvt_pk_bf16_f32 v227, v198, v199\n" \
    "v_exp_f32 v200, v200\n" \
    "v_exp_f32 v201, v201\n" \
    "v_exp_f32 v202, v202\n" \
    "v_cvt_pk_bf16_f32 v228, v200, v201\n" \
    "v_exp_f32 v203, v203\n" \
    "v_mfma_f32_32x32x16_bf16 v[112:127], v[172:175], v[236:239], v[112:127]\n" \
    "v_cvt_pk_bf16_f32 v229, v202, v203\n" \
    "v_exp_f32 v204, v204\n" \
    "v_exp_f32 v205, v205\n" \
    "v_exp_f32 v206, v206\n" \
    "v_cvt_pk_bf16_f32 v230, v204, v205\n" \
    "v_exp_f32 v207, v207\n" \
    "v_mfma_f32_32x32x16_bf16 v[64:79], v[184:187], v[224:227], v[64:79]\n" \
    "buffer_load_dwordx2 v[160:161], v2, %[ratt], s66 offen offset:0 sc1\n" \
    "buffer_load_dwordx2 v[162:163], v2, %[ratt], s66 offen offset:512 sc1\n" \
    "buffer_load_dwordx2 v[164:165], v2, %[ratt], s66 offen offset:1024 sc1\n" \
    "buffer_load_dwordx2 v[166:167], v2, %[ratt], s66 offen offset:1536 sc1\n" \
    "buffer_load_dwordx4 v[168:171], v1, %[ratt], s66 offen offset:2048 sc1\n" \
    "buffer_load_dwordx4 v[172:175], v1, %[ratt], s66 offen offset:3072 sc1\n" \
    "v_cvt_pk_bf16_f32 v231, v206, v207\n" \
    "v_exp_f32 v208, v208\n" \
    "v_exp_f32 v209, v209\n" \
    "v_exp_f32 v210, v210\n" \
    "v_cvt_pk_bf16_f32 v232, v208, v209\n" \
    "v_exp_f32 v211, v211\n" \
    "v_mfma_f32_32x32x16_bf16 v[192:207], v[176:179], v[48:51], 0\n" \
    "s_add_u32 s66, s66, 0x1000\n" \
    "v_cvt_pk_bf16_f32 v233, v210, v211\n" \
    "v_exp_f32 v212, v212\n" \
    "v_exp_f32 v213, v213\n" \
    "v_exp_f32 v214, v214\n" \
    "v_cvt_pk_bf16_f32 v234, v212, v213\n" \
    "v_exp_f32 v215, v215\n" \
    "v_mfma_f32_32x32x16_bf16 v[192:207], v[180:183], v[52:55], v[192:207]\n" \
    "v_cvt_pk_bf16_f32 v235, v214, v215\n" \
    "v_exp_f32 v216, v216\n" \
    "v_exp_f32 v217, v217\n" \
    "v_exp_f32 v218, v218\n" \
    "v_cvt_pk_bf16_f32 v236, v216, v217\n" \
    "v_exp_f32 v219, v219\n" \
    "v_mfma_f32_32x32x16_bf16 v[64:79], v[188:191], v[228:231], v[64:79]\n" \
    "v_cvt_pk_bf16_f32 v237, v218, v219\n" \
    "v_exp_f32 v220, v220\n" \
    "v_exp_f32 v221, v221\n" \
    "v_exp_f32 v222, v222\n" \
    "v_cvt_pk_bf16_f32 v238, v220, v221\n" \
    "v_exp_f32 v223, v223\n" \
    "v_mfma_f32_32x32x16_bf16 v[80:95], v[184:187], v[232:235], v[80:95]\n" \
    "v_cvt_pk_bf16_f32 v239, v222, v223\n" \
    "v_exp_f32 v192, v192\n" \
    "v_exp_f32 v193, v193\n" \
    "v_exp_f32 v194, v194\n" \
    "v_cvt_pk_bf16_f32 v224, v192, v193\n" \
    "v_exp_f32 v195, v195\n" \
    "v_mfma_f32_32x32x16_bf16 v[208:223], v[176:179], v[56:59], 0\n" \
    "v_cvt_pk_bf16_f32 v225, v194, v195\n" \
    "v_exp_f32 v196, v196\n" \
    "v_exp_f32 v197, v197\n" \
    "v_exp_f32 v198, v198\n" \
    "v_cvt_pk_bf16_f32 v226, v196, v197\n" \
    "v_exp_f32 v199, v199\n" \
    "v_mfma_f32_32x32x16_bf16 v[208:223], v[180:183], v[60:63], v[208:223]\n" \
    "v_cvt_pk_bf16_f32 v227, v198, v199\n" \
    "v_exp_f32 v200, v200\n" \
    "v_exp_f32 v201, v201\n" \
    "v_exp_f32 v202, v202\n" \
    "v_cvt_pk_bf16_f32 v228, v200, v201\n" \
    "v_exp_f32 v203, v203\n" \
    "v_mfma_f32_32x32x16_bf16 v[80:95], v[188:191], v[236:239], v[80:95]\n" \
    "v_cvt_pk_bf16_f32 v229, v202, v203\n" \
    "v_exp_f32 v204, v204\n" \
    "v_exp_f32 v205, v205\n" \
    "v_exp_f32 v206, v206\n" \
    "v_cvt_pk_bf16_f32 v230, v204, v205\n" \
    "v_exp_f32 v207, v207\n" \
    "v_mfma_f32_32x32x16_bf16 v[96:111], v[184:187], v[224:227], v[96:111]\n" \
    "s_waitcnt vmcnt(12)\n" \
    "v_cvt_pk_bf16_f32 v231, v206, v207\n" \
    "v_exp_f32 v208, v208\n" \
    "v_exp_f32 v209, v209\n" \
    "v_exp_f32 v210, v210\n" \
    "v_cvt_pk_bf16_f32 v232, v208, v209\n" \
    "v_exp_f32 v211, v211\n" \
    "v_mfma_f32_32x32x16_bf16 v[192:207], v[128:131], v[32:35], 0\n" \
    "v_cvt_pk_bf16_f32 v233, v210, v211\n" \
    "v_exp_f32 v212, v212\n" \
    "v_exp_f32 v213, v213\n" \
    "v_exp_f32 v214, v214\n" \
    "v_cvt_pk_bf16_f32 v234, v212, v213\n" \
    "v_exp_f32 v215, v215\n" \
    "v_mfma_f32_32x32x16_bf16 v[192:207], v[132:135], v[36:39], v[192:207]\n" \
    "v_cvt_pk_bf16_f32 v235, v214, v215\n" \
    "v_exp_f32 v216, v216\n" \
    "v_exp_f32 v217, v217\n" \
    "v_exp_f32 v218, v218\n" \
    "v_cvt_pk_bf16_f32 v236, v216, v217\n" \
    "v_exp_f32 v219, v219\n" \
    "v_mfma_f32_32x32x16_bf16 v[96:111], v[188:191], v[228:231], v[96:111]\n" \
    "v_cvt_pk_bf16_f32 v237, v218, v219\n" \
    "v_exp_f32 v220, v220\n" \
    "v_exp_f32 v221, v221\n" \
    "v_exp_f32 v222, v222\n" \
    "v_cvt_pk_bf16_f32 v238, v220, v221\n" \
    "v_exp_f32 v223, v223\n" \
    "s_branch MN_LOOP_%=\n" \
    "MN_PEEL_%=:\n" \
    "s_cmp_lg_u32 s70, 0\n" \
    "s_cbranch_scc1 MN_NOB5_%=\n" \
    "s_barrier\n" \
    "MN_NOB5_%=:\n" \
    "s_setprio 3\n" \
    "v_mfma_f32_32x32x16_bf16 v[112:127], v[168:171], v[232:235], v[112:127]\n" \
    "v_cvt_pk_bf16_f32 v239, v222, v223\n" \
    "v_exp_f32 v192, v192\n" \
    "v_exp_f32 v193, v193\n" \
    "v_exp_f32 v194, v194\n" \
    "v_cvt_pk_bf16_f32 v224, v192, v193\n" \
    "v_exp_f32 v195, v195\n" \
    "v_mfma_f32_32x32x16_bf16 v[208:223], v[176:179], v[40:43], 0\n" \
    "v_cvt_pk_bf16_f32 v225, v194, v195\n" \
    "v_exp_f32 v196, v196\n" \
    "v_exp_f32 v197, v197\n" \
    "v_exp_f32 v198, v198\n" \
    "v_cvt_pk_bf16_f32 v226, v196, v197\n" \
    "v_exp_f32 v199, v199\n" \
    "v_mfma_f32_32x32x16_bf16 v[208:223], v[180:183], v[44:47], v[208:223]\n" \
    "v_cvt_pk_bf16_f32 v227, v198, v199\n" \
    "v_exp_f32 v200, v200\n" \
    "v_exp_f32 v201, v201\n" \
    "v_exp_f32 v202, v202\n" \
    "v_cvt_pk_bf16_f32 v228, v200, v201\n" \
    "v_exp_f32 v203, v203\n" \
    "v_mfma_f32_32x32x16_bf16 v[112:127], v[172:175], v[236:239], v[112:127]\n" \
    "v_cvt_pk_bf16_f32 v229, v202, v203\n" \
    "v_exp_f32 v204, v204\n" \
    "v_exp_f32 v205, v205\n" \
    "v_exp_f32 v206, v206\n" \
    "v_cvt_pk_bf16_f32 v230, v204, v205\n" \
    "v_exp_f32 v207, v207\n" \
    "v_mfma_f32_32x32x16_bf16 v[64:79], v[184:187], v[224:227], v[64:79]\n" \
    "v_cvt_pk_bf16_f32 v231, v206, v207\n" \
    "v_exp_f32 v208, v208\n" \
    "v_exp_f32 v209, v209\n" \
    "v_exp_f32 v210, v210\n" \
    "v_cvt_pk_bf16_f32 v232, v208, v209\n" \
    "v_exp_f32 v211, v211\n" \
    "v_mfma_f32_32x32x16_bf16 v[192:207], v[176:179], v[48:51], 0\n" \
    "v_cvt_pk_bf16_f32 v233, v210, v211\n" \
    "v_exp_f32 v212, v212\n" \
    "v_exp_f32 v213, v213\n" \
    "v_exp_f32 v214, v214\n" \
    "v_cvt_pk_bf16_f32 v234, v212, v213\n" \
    "v_exp_f32 v215, v215\n" \
    "v_mfma_f32_32x32x16_bf16 v[192:207], v[180:183], v[52:55], v[192:207]\n" \
    "v_cvt_pk_bf16_f32 v235, v214, v215\n" \
    "v_exp_f32 v216, v216\n" \
    "v_exp_f32 v217, v217\n" \
    "v_exp_f32 v218, v218\n" \
    "v_cvt_pk_bf16_f32 v236, v216, v217\n" \
    "v_exp_f32 v219, v219\n" \
    "v_mfma_f32_32x32x16_bf16 v[64:79], v[188:191], v[228:231], v[64:79]\n" \
    "v_cvt_pk_bf16_f32 v237, v218, v219\n" \
    "v_exp_f32 v220, v220\n" \
    "v_exp_f32 v221, v221\n" \
    "v_exp_f32 v222, v222\n" \
    "v_cvt_pk_bf16_f32 v238, v220, v221\n" \
    "v_exp_f32 v223, v223\n" \
    "v_mfma_f32_32x32x16_bf16 v[80:95], v[184:187], v[232:235], v[80:95]\n" \
    "v_cvt_pk_bf16_f32 v239, v222, v223\n" \
    "v_exp_f32 v192, v192\n" \
    "v_exp_f32 v193, v193\n" \
    "v_exp_f32 v194, v194\n" \
    "v_cvt_pk_bf16_f32 v224, v192, v193\n" \
    "v_exp_f32 v195, v195\n" \
    "v_mfma_f32_32x32x16_bf16 v[208:223], v[176:179], v[56:59], 0\n" \
    "ds_write_b128 v28, v[64:67] offset:0\n" \
    "v_cndmask_b32_e64 v77, v77, v240, s[76:77]\n" \
    "v_cvt_pk_bf16_f32 v225, v194, v195\n" \
    "v_exp_f32 v196, v196\n" \
    "v_exp_f32 v197, v197\n" \
    "v_exp_f32 v198, v198\n" \
    "v_cvt_pk_bf16_f32 v226, v196, v197\n" \
    "v_exp_f32 v199, v199\n" \
    "v_mfma_f32_32x32x16_bf16 v[208:223], v[180:183], v[60:63], v[208:223]\n" \
    "ds_write_b128 v28, v[68:71] offset:1024\n" \
    "v_cvt_pk_bf16_f32 v227, v198, v199\n" \
    "v_exp_f32 v200, v200\n" \
    "v_exp_f32 v201, v201\n" \
    "v_exp_f32 v202, v202\n" \
    "v_cvt_pk_bf16_f32 v228, v200, v201\n" \
    "v_exp_f32 v203, v203\n" \
    "v_mfma_f32_32x32x16_bf16 v[80:95], v[188:191], v[236:239], v[80:95]\n" \
    "ds_write_b128 v28, v[72:75] offset:2048\n" \
    "v_cvt_pk_bf16_f32 v229, v202, v203\n" \
    "v_exp_f32 v204, v204\n" \
    "v_exp_f32 v205, v205\n" \
    "v_exp_f32 v206, v206\n" \
    "v_cvt_pk_bf16_f32 v230, v204, v205\n" \
    "v_exp_f32 v207, v207\n" \
    "v_mfma_f32_32x32x16_bf16 v[96:111], v[184:187], v[224:227], v[96:111]\n" \
    "ds_write_b128 v28, v[76:79] offset:3072\n" \
    "v_cvt_pk_bf16_f32 v231, v206, v207\n" \
    "v_exp_f32 v208, v208\n" \
    "v_exp_f32 v209, v209\n" \
    "v_exp_f32 v210, v210\n" \
    "v_cvt_pk_bf16_f32 v232, v208, v209\n" \
    "v_exp_f32 v211, v211\n" \
    "ds_write_b128 v29, v[80:83] offset:0\n" \
    "v_cndmask_b32_e64 v93, v93, v241, s[76:77]\n" \
    "v_cvt_pk_bf16_f32 v233, v210, v211\n" \
    "v_exp_f32 v212, v212\n" \
    "v_exp_f32 v213, v213\n" \
    "v_exp_f32 v214, v214\n" \
    "v_cvt_pk_bf16_f32 v234, v212, v213\n" \
    "v_exp_f32 v215, v215\n" \
    "ds_write_b128 v29, v[84:87] offset:1024\n" \
    "v_cvt_pk_bf16_f32 v235, v214, v215\n" \
    "v_exp_f32 v216, v216\n" \
    "v_exp_f32 v217, v217\n" \
    "v_exp_f32 v218, v218\n" \
    "v_cvt_pk_bf16_f32 v236, v216, v217\n" \
    "v_exp_f32 v219, v219\n" \
    "v_mfma_f32_32x32x16_bf16 v[96:111], v[188:191], v[228:231], v[96:111]\n" \
    "ds_write_b128 v29, v[88:91] offset:2048\n" \
    "v_cvt_pk_bf16_f32 v237, v218, v219\n" \
    "v_exp_f32 v220, v220\n" \
    "v_exp_f32 v221, v221\n" \
    "v_exp_f32 v222, v222\n" \
    "v_cvt_pk_bf16_f32 v238, v220, v221\n" \
    "v_exp_f32 v223, v223\n" \
    "v_mfma_f32_32x32x16_bf16 v[112:127], v[184:187], v[232:235], v[112:127]\n" \
    "ds_write_b128 v29, v[92:95] offset:3072\n" \
    "v_cvt_pk_bf16_f32 v239, v222, v223\n" \
    "v_min_f32 v244, v76, v92\n" \
    "v_max_f32 v245, v76, v92\n" \
    "v_mfma_f32_32x32x16_bf16 v[112:127], v[188:191], v[236:239], v[112:127]\n" \
    "s_mov_b32 s61, 0x21800000\n" \
    "s_mov_b32 s72, 0x5d800000\n" \
    "v_cndmask_b32_e64 v109, v109, v242, s[76:77]\n" \
    "ds_write_b128 v30, v[108:111] offset:3072\n" \
    "ds_write_b128 v30, v[96:99] offset:0\n" \
    "ds_write_b128 v30, v[100:103] offset:1024\n" \
    "ds_write_b128 v30, v[104:107] offset:2048\n" \
    "s_waitcnt vmcnt(0)\n" \
    "s_nop 4\n" \
    "v_min3_f32 v244, v244, v108, v124\n" \
    "v_max3_f32 v245, v245, v108, v124\n" \
    "v_cmp_nlt_f32 s[74:75], s61, v244\n" \
    "v_cmp_ngt_f32 vcc, s72, v245\n" \
    "s_or_b32 s75, s75, vcc_hi\n" \
    "s_cbranch_scc0 MN_ATTD_%=\n" \
    "s_cmp_lg_u32 s70, 0\n" \
    "s_cbranch_scc1 MN_ATTD_%=\n" \
    "s_mov_b32 s70, 1\n" \
    "s_setprio 0\n" \
    "s_waitcnt lgkmcnt(0)\n" \
    "v_mov_b32 v184, 0\n" \
    "v_mov_b32 v185, 0\n" \
    "v_mov_b32 v186, 0\n" \
    "v_mov_b32 v187, 0\n" \
    "v_mov_b32 v188, 0\n" \
    "v_mov_b32 v189, 0\n" \
    "v_mov_b32 v190, 0\n" \
    "v_mov_b32 v191, 0\n" \
    "v_mov_b32 v232, 0\n" \
    "v_mov_b32 v233, 0\n" \
    "v_mov_b32 v234, 0\n" \
    "v_mov_b32 v235, 0\n" \
    "v_mov_b32 v236, 0\n" \
    "v_mov_b32 v237, 0\n" \
    "v_mov_b32 v238, 0\n" \
    "v_mov_b32 v239, 0\n" \
    "v_mov_b32 v240, 0xff800000\n" \
    "v_mov_b32 v241, 0xff800000\n" \
    "v_mov_b32 v242, 0xff800000\n" \
    "v_mov_b32 v243, 0xff800000\n" \
    "v_mfma_f32_32x32x16_bf16 v[64:79], v[184:187], v[184:187], 0\n" \
    "v_mfma_f32_32x32x16_bf16 v[80:95], v[184:187], v[184:187], 0\n" \
    "v_mfma_f32_32x32x16_bf16 v[96:111], v[184:187], v[184:187], 0\n" \
    "v_mfma_f32_32x32x16_bf16 v[112:127], v[184:187], v[184:187], 0\n" \
    "buffer_load_dwordx2 v[128:129], v2, %[ratt], 0 offen offset:0 sc1\n" \
    "buffer_load_dwordx2 v[130:131], v2, %[ratt], 0 offen offset:512 sc1\n" \
    "buffer_load_dwordx2 v[132:133], v2, %[ratt], 0 offen offset:1024 sc1\n" \
    "buffer_load_dwordx2 v[134:135], v2, %[ratt], 0 offen offset:1536 sc1\n" \
    "buffer_load_dwordx4 v[136:139], v1, %[ratt], 0 offen offset:2048 sc1\n" \
    "buffer_load_dwordx4 v[140:143], v1, %[ratt], 0 offen offset:3072 sc1\n" \
    "s_mov_b32 s66, 0x1000\n" \
    "s_mov_b32 s68, 0\n" \
    "s_branch MN_ATT_%=\n" \
    "MN_MAXT_%=:\n" \
    "v_permlane32_swap_b32 v243, v247\n" \
    "s_add_u32 s68, s68, 0x1000\n" \
    "v_max_f32 v243, v243, v247\n" \
    "s_and_b32 s68, s68, 0xffff\n" \
    "buffer_load_dwordx2 v[128:129], v2, %[ratt], s68 offen offset:0 sc1\n" \
    "buffer_load_dwordx2 v[130:131], v2, %[ratt], s68 offen offset:512 sc1\n" \
    "buffer_load_dwordx2 v[132:133], v2, %[ratt], s68 offen offset:1024 sc1\n" \
    "buffer_load_dwordx2 v[134:135], v2, %[ratt], s68 offen offset:1536 sc1\n" \
    "s_waitcnt vmcnt(0)\n" \
    "s_cmp_lg_u32 s68, 0\n" \
    "s_cbranch_scc1 MN_MAXL_%=\n" \
    "s_add_u32 s66, s66, 0x1000\n" \
    "buffer_load_dwordx2 v[160:161], v2, %[ratt], s66 offen offset:0 sc1\n" \
    "buffer_load_dwordx2 v[162:163], v2, %[ratt], s66 offen offset:512 sc1\n" \
    "buffer_load_dwordx2 v[164:165], v2, %[ratt], s66 offen offset:1024 sc1\n" \
    "buffer_load_dwordx2 v[166:167], v2, %[ratt], s66 offen offset:1536 sc1\n" \
    "buffer_load_dwordx4 v[168:171], v1, %[ratt], s66 offen offset:2048 sc1\n" \
    "buffer_load_dwordx4 v[172:175], v1, %[ratt], s66 offen offset:3072 sc1\n" \
    "s_mov_b32 exec_lo, 0\n" \
    "v_cvt_pk_bf16_f32 v208, -v240, -v240\n" \
    "v_cvt_pk_bf16_f32 v209, -v241, -v241\n" \
    "v_cvt_pk_bf16_f32 v210, -v242, -v242\n" \
    "v_cvt_pk_bf16_f32 v211, -v243, -v243\n" \
    "v_lshlrev_b32 v208, 16, v208\n" \
    "v_lshlrev_b32 v209, 16, v209\n" \
    "v_lshlrev_b32 v210, 16, v210\n" \
    "v_lshlrev_b32 v211, 16, v211\n" \
    "v_sub_f32_e64 v212, -v240, v208\n" \
    "v_sub_f32_e64 v213, -v241, v209\n" \
    "v_sub_f32_e64 v214, -v242, v210\n" \
    "v_sub_f32_e64 v215, -v243, v211\n" \
    "v_cvt_pk_bf16_f32 v38, -v240, v212\n" \
    "v_cvt_pk_bf16_f32 v46, -v241, v213\n" \
    "v_cvt_pk_bf16_f32 v54, -v242, v214\n" \
    "v_cvt_pk_bf16_f32 v62, -v243, v215\n" \
    "v_and_b32 v208, 0xffff0000, v38\n" \
    "v_and_b32 v209, 0xffff0000, v46\n" \
    "v_and_b32 v210, 0xffff0000, v54\n" \
    "v_and_b32 v211, 0xffff0000, v62\n" \
    "v_sub_f32 v212, v212, v208\n" \
    "v_sub_f32 v213, v213, v209\n" \
    "v_sub_f32 v214, v214, v210\n" \
    "v_sub_f32 v215, v215, v211\n" \
    "v_cvt_pk_bf16_f32 v39, v212, 0\n" \
    "v_cvt_pk_bf16_f32 v47, v213, 0\n" \
    "v_cvt_pk_bf16_f32 v55, v214, 0\n" \
    "v_cvt_pk_bf16_f32 v63, v215, 0\n" \
    "s_mov_b32 exec_lo, -1\n" \
    "v_mfma_f32_32x32x16_bf16 v[192:207], v[128:131], v[32:35], 0\n" \
    "v_mfma_f32_32x32x16_bf16 v[192:207], v[132:135], v[36:39], v[192:207]\n" \
    "s_nop 7\n" \
    "s_branch MN_GO_%=\n" \
    "MN_ATTD_%=:\n" \
    "s_setprio 3\n" \
    "s_waitcnt lgkmcnt(0)\n" \
    "s_barrier\n" \
    "ds_read_b128 v[140:143], v31 offset:3072\n" \
    "ds_read_b128 v[156:159], v31 offset:7168\n" \
    "ds_read_b128 v[172:175], v31 offset:11264\n" \
    "ds_read_b128 v[128:131], v31 offset:0\n" \
    "ds_read_b128 v[144:147], v31 offset:4096\n" \
    "ds_read_b128 v[160:163], v31 offset:8192\n" \
    "ds_read_b128 v[132:135], v31 offset:1024\n" \
    "ds_read_b128 v[148:151], v31 offset:5120\n" \
    "ds_read_b128 v[164:167], v31 offset:9216\n" \
    "ds_read_b128 v[136:139], v31 offset:2048\n" \
    "ds_read_b128 v[152:155], v31 offset:6144\n" \
    "ds_read_b128 v[168:171], v31 offset:10240\n" \
    "s_waitcnt lgkmcnt(9)\n" \
    "v_mov_b32 v192, v141\n" \
    "v_mov_b32 v193, v141\n" \
    "v_mov_b32 v194, v157\n" \
    "v_mov_b32 v195, v157\n" \
    "v_mov_b32 v196, v173\n" \
    "v_mov_b32 v197, v173\n" \
    "v_permlane32_swap_b32 v193, v192\n" \
    "v_permlane32_swap_b32 v195, v194\n" \
    "v_permlane32_swap_b32 v197, v196\n" \
    "v_max3_f32 v3, v192, v194, v196\n" \
    "v_max_f32 v3, v3, v243\n" \
    "v_sub_f32 v192, v192, v3\n" \
    "v_sub_f32 v194, v194, v3\n" \
    "v_sub_f32 v196, v196, v3\n" \
    "v_sub_f32 v198, v243, v3\n" \
    "v_exp_f32 v192, v192\n" \
    "v_exp_f32 v194, v194\n" \
    "v_exp_f32 v196, v196\n" \
    "v_exp_f32 v198, v198\n" \
    "s_nop 0\n" \
    "v_mul_f32 v3, v198, v124\n" \
    "v_fmac_f32 v3, v192, v140\n" \
    "v_fmac_f32 v3, v194, v156\n" \
    "v_fmac_f32 v3, v196, v172\n" \
    "v_mov_b32 v9, v3\n" \
    "s_nop 1\n" \
    "v_permlane32_swap_b32 v9, v3\n" \
    "v_rcp_f32 v3, v3\n" \
    "s_nop 0\n" \
    "v_mul_f32 v192, v192, v3\n" \
    "v_mul_f32 v194, v194, v3\n" \
    "v_mul_f32 v196, v196, v3\n" \
    "v_mul_f32 v198, v198, v3\n" \
    "s_lshl_b32 s61, %[t], 2\n" \
    "s_add_u32 s61, s61, %[wv]\n" \
    "s_lshl_b32 s61, s61, 4\n" \
    "s_add_u32 s61, s61, %[idx]\n" \
    "s_lshl_b32 s61, s61, 11\n" \
    "v_pk_mul_f32 v[112:113], v[198:199], v[112:113] op_sel_hi:[0,1]\n" \
    "v_pk_mul_f32 v[114:115], v[198:199], v[114:115] op_sel_hi:[0,1]\n" \
    "v_pk_mul_f32 v[116:117], v[198:199], v[116:117] op_sel_hi:[0,1]\n" \
    "v_pk_mul_f32 v[118:119], v[198:199], v[118:119] op_sel_hi:[0,1]\n" \
    "s_waitcnt lgkmcnt(5)\n" \
    "v_pk_fma_f32 v[112:113], v[192:193], v[128:129], v[112:113] op_sel_hi:[0,1,1]\n" \
    "v_pk_fma_f32 v[114:115], v[192:193], v[130:131], v[114:115] op_sel_hi:[0,1,1]\n" \
    "v_pk_fma_f32 v[116:117], v[192:193], v[132:133], v[116:117] op_sel_hi:[0,1,1]\n" \
    "v_pk_fma_f32 v[118:119], v[192:193], v[134:135], v[118:119] op_sel_hi:[0,1,1]\n" \
    "s_waitcnt lgkmcnt(4)\n" \
    "v_pk_fma_f32 v[112:113], v[194:195], v[144:145], v[112:113] op_sel_hi:[0,1,1]\n" \
    "v_pk_fma_f32 v[114:115], v[194:195], v[146:147], v[114:115] op_sel_hi:[0,1,1]\n" \
    "v_pk_fma_f32 v[116:117], v[194:195], v[148:149], v[116:117] op_sel_hi:[0,1,1]\n" \
    "v_pk_fma_f32 v[118:119], v[194:195], v[150:151], v[118:119] op_sel_hi:[0,1,1]\n" \
    "s_waitcnt lgkmcnt(3)\n" \
    "v_pk_fma_f32 v[112:113], v[196:197], v[160:161], v[112:113] op_sel_hi:[0,1,1]\n" \
    "v_pk_fma_f32 v[114:115], v[196:197], v[162:163], v[114:115] op_sel_hi:[0,1,1]\n" \
    "v_pk_fma_f32 v[116:117], v[196:197], v[164:165], v[116:117] op_sel_hi:[0,1,1]\n" \
    "v_pk_fma_f32 v[118:119], v[196:197], v[166:167], v[118:119] op_sel_hi:[0,1,1]\n" \
    "v_cvt_pk_bf16_f32 v200, v112, v113\n" \
    "v_cvt_pk_bf16_f32 v201, v114, v115\n" \
    "v_cvt_pk_bf16_f32 v202, v116, v117\n" \
    "v_cvt_pk_bf16_f32 v203, v118, v119\n" \
    "buffer_store_dwordx4 v[200:203], v1, %[ro], s61 offen offset:0 sc1\n" \
    "v_pk_mul_f32 v[120:121], v[198:199], v[120:121] op_sel_hi:[0,1]\n" \
    "v_pk_mul_f32 v[122:123], v[198:199], v[122:123] op_sel_hi:[0,1]\n" \
    "v_pk_mul_f32 v[124:125], v[198:199], v[124:125] op_sel_hi:[0,1]\n" \
    "v_pk_mul_f32 v[126:127], v[198:199], v[126:127] op_sel_hi:[0,1]\n" \
    "s_waitcnt lgkmcnt(2)\n" \
    "v_pk_fma_f32 v[120:121], v[192:193], v[136:137], v[120:121] op_sel_hi:[0,1,1]\n" \
    "v_pk_fma_f32 v[122:123], v[192:193], v[138:139], v[122:123] op_sel_hi:[0,1,1]\n" \
    "v_pk_fma_f32 v[124:125], v[192:193], v[140:141], v[124:125] op_sel_hi:[0,1,1]\n" \
    "v_pk_fma_f32 v[126:127], v[192:193], v[142:143], v[126:127] op_sel_hi:[0,1,1]\n" \
    "s_waitcnt lgkmcnt(1)\n" \
    "v_pk_fma_f32 v[120:121], v[194:195], v[152:153], v[120:121] op_sel_hi:[0,1,1]\n" \
    "v_pk_fma_f32 v[122:123], v[194:195], v[154:155], v[122:123] op_sel_hi:[0,1,1]\n" \
    "v_pk_fma_f32 v[124:125], v[194:195], v[156:157], v[124:125] op_sel_hi:[0,1,1]\n" \
    "v_pk_fma_f32 v[126:127], v[194:195], v[158:159], v[126:127] op_sel_hi:[0,1,1]\n" \
    "s_waitcnt lgkmcnt(0)\n" \
    "v_pk_fma_f32 v[120:121], v[196:197], v[168:169], v[120:121] op_sel_hi:[0,1,1]\n" \
    "v_pk_fma_f32 v[122:123], v[196:197], v[170:171], v[122:123] op_sel_hi:[0,1,1]\n" \
    "v_pk_fma_f32 v[124:125], v[196:197], v[172:173], v[124:125] op_sel_hi:[0,1,1]\n" \
    "v_pk_fma_f32 v[126:127], v[196:197], v[174:175], v[126:127] op_sel_hi:[0,1,1]\n" \
    "v_cvt_pk_bf16_f32 v204, v120, v121\n" \
    "v_cvt_pk_bf16_f32 v205, v122, v123\n" \
    "v_cvt_pk_bf16_f32 v206, v124, v125\n" \
    "v_cvt_pk_bf16_f32 v207, v126, v127\n" \
    "s_mov_b32 exec_lo, 0\n" \
    "v_mov_b32 v206, 0\n" \
    "v_mov_b32 v207, 0\n" \
    "s_mov_b32 exec_lo, -1\n" \
    "buffer_store_dwordx4 v[204:207], v1, %[ro], s61 offen offset:1024 sc1\n" \
    "s_setprio 0\n" \
    "s_cmp_lt_u32 %[idx], 14\n" \
    "s_cbranch_scc0 MN_END_%=\n" \
    "buffer_load_dwordx4 v[12:15], v250, %[rx], 0 offen\n" \
    "v_mov_b32 v10, 163836\n" \
    "v_mov_b32 v11, 0\n" \
    "s_waitcnt lgkmcnt(0)\n" \
    "s_cmp_lg_u32 %[wv], 3\n" \
    "s_cbranch_scc1 MN_NOZ_%=\n" \
    "ds_write_b32 v10, v11\n" \
    "MN_NOZ_%=:\n" \
    "s_waitcnt lgkmcnt(0)\n" \
    "s_barrier\n" \
    "s_sub_u32 s61, %[wv], %[tt]\n" \
    "s_cmp_eq_u32 %[ct], 3\n" \
    "s_cselect_b32 s72, 2, 1\n" \
    "s_cmp_lt_u32 s61, s72\n" \
    "s_cbranch_scc0 MN_NOG_%=\n" \
    "s_lshl_b32 s61, s61, 7\n" \
    "s_add_u32 s61, s61, 156544\n" \
    "s_mov_b32 exec_lo, 0\n" \
    "v_lshlrev_b32 v3, 2, v0\n" \
    "v_add_u32 v3, s61, v3\n" \
    "ds_write_b32 v3, v8\n" \
    "s_mov_b32 exec_lo, -1\n" \
    "MN_NOG_%=:\n" \
    "v_sub_u32 v253, v248, v253\n" \
    "s_cmp_eq_u32 %[ct], 3\n" \
    "s_cbranch_scc1 MN_WF3_%=\n" \
    "v_add_u32 v3, 57344, v248\n" \
    "v_add_u32 v253, 57344, v253\n" \
    "ds_read_b64_tr_b16 v[120:121], v248 offset:0\n" \
    "ds_read_b64_tr_b16 v[122:123], v248 offset:2048\n" \
    "ds_read_b64_tr_b16 v[124:125], v248 offset:4096\n" \
    "ds_read_b64_tr_b16 v[126:127], v248 offset:6144\n" \
    "ds_read_b64_tr_b16 v[128:129], v248 offset:7168\n" \
    "ds_read_b64_tr_b16 v[130:131], v248 offset:9216\n" \
    "ds_read_b64_tr_b16 v[132:133], v248 offset:11264\n" \
    "ds_read_b64_tr_b16 v[134:135], v248 offset:13312\n" \
    "ds_read_b64_tr_b16 v[136:137], v248 offset:14336\n" \
    "ds_read_b64_tr_b16 v[138:139], v248 offset:16384\n" \
    "ds_read_b64_tr_b16 v[140:141], v248 offset:18432\n" \
    "ds_read_b64_tr_b16 v[142:143], v248 offset:20480\n" \
    "ds_read_b64_tr_b16 v[144:145], v248 offset:21504\n" \
    "ds_read_b64_tr_b16 v[146:147], v248 offset:23552\n" \
    "ds_read_b64_tr_b16 v[148:149], v248 offset:25600\n" \
    "ds_read_b64_tr_b16 v[150:151], v248 offset:27648\n" \
    "ds_read_b64_tr_b16 v[152:153], v248 offset:28672\n" \
    "ds_read_b64_tr_b16 v[154:155], v248 offset:30720\n" \
    "ds_read_b64_tr_b16 v[156:157], v248 offset:32768\n" \
    "ds_read_b64_tr_b16 v[158:159], v248 offset:34816\n" \
    "ds_read_b64_tr_b16 v[160:161], v248 offset:35840\n" \
    "ds_read_b64_tr_b16 v[162:163], v248 offset:37888\n" \
    "ds_read_b64_tr_b16 v[164:165], v248 offset:39936\n" \
    "ds_read_b64_tr_b16 v[166:167], v248 offset:41984\n" \
    "ds_read_b64_tr_b16 v[168:169], v248 offset:43008\n" \
    "ds_read_b64_tr_b16 v[170:171], v248 offset:45056\n" \
    "ds_read_b64_tr_b16 v[172:173], v248 offset:47104\n" \
    "ds_read_b64_tr_b16 v[174:175], v248 offset:49152\n" \
    "ds_read_b64_tr_b16 v[176:177], v248 offset:50176\n" \
    "ds_read_b64_tr_b16 v[178:179], v248 offset:52224\n" \
    "ds_read_b64_tr_b16 v[180:181], v248 offset:54272\n" \
    "ds_read_b64_tr_b16 v[182:183], v248 offset:56320\n" \
    "ds_read_b64_tr_b16 v[184:185], v3 offset:0\n" \
    "ds_read_b64_tr_b16 v[186:187], v3 offset:2048\n" \
    "ds_read_b64_tr_b16 v[188:189], v3 offset:4096\n" \
    "ds_read_b64_tr_b16 v[190:191], v3 offset:6144\n" \
    "ds_read_b64_tr_b16 v[192:193], v3 offset:7168\n" \
    "ds_read_b64_tr_b16 v[194:195], v3 offset:9216\n" \
    "ds_read_b64_tr_b16 v[196:197], v3 offset:11264\n" \
    "ds_read_b64_tr_b16 v[198:199], v3 offset:13312\n" \
    "ds_read_b64_tr_b16 v[200:201], v3 offset:14336\n" \
    "ds_read_b64_tr_b16 v[202:203], v3 offset:16384\n" \
    "ds_read_b64_tr_b16 v[204:205], v3 offset:18432\n" \
    "ds_read_b64_tr_b16 v[206:207], v3 offset:20480\n" \
    "ds_read_b64_tr_b16 v[208:209], v3 offset:21504\n" \
    "ds_read_b64_tr_b16 v[210:211], v3 offset:23552\n" \
    "ds_read_b64_tr_b16 v[212:213], v3 offset:25600\n" \
    "ds_read_b64_tr_b16 v[214:215], v3 offset:27648\n" \
    "ds_read_b64_tr_b16 v[216:217], v3 offset:28672\n" \
    "ds_read_b64_tr_b16 v[218:219], v3 offset:30720\n" \
    "ds_read_b64_tr_b16 v[220:221], v3 offset:32768\n" \
    "ds_read_b64_tr_b16 v[222:223], v3 offset:34816\n" \
    "ds_read_b64_tr_b16 v[224:225], v3 offset:35840\n" \
    "ds_read_b64_tr_b16 v[226:227], v3 offset:37888\n" \
    "ds_read_b64_tr_b16 v[228:229], v3 offset:39936\n" \
    "ds_read_b64_tr_b16 v[230:231], v3 offset:41984\n" \
    "ds_read_b64_tr_b16 v[232:233], v3 offset:43008\n" \
    "ds_read_b64_tr_b16 v[234:235], v3 offset:45056\n" \
    "ds_read_b64_tr_b16 v[236:237], v3 offset:47104\n" \
    "ds_read_b64_tr_b16 v[238:239], v3 offset:49152\n" \
    "ds_read_b64_tr_b16 v[240:241], v3 offset:50176\n" \
    "ds_read_b64_tr_b16 v[242:243], v3 offset:52224\n" \
    "ds_read_b64_tr_b16 v[244:245], v3 offset:54272\n" \
    "ds_read_b64_tr_b16 v[246:247], v253 offset:56320\n" \
    "s_branch MN_WFD_%=\n" \
    "MN_WF3_%=:\n" \
    "ds_read_b64_tr_b16 v[120:121], v248 offset:0\n" \
    "ds_read_b64_tr_b16 v[122:123], v248 offset:1024\n" \
    "ds_read_b64_tr_b16 v[124:125], v248 offset:2048\n" \
    "ds_read_b64_tr_b16 v[126:127], v248 offset:3072\n" \
    "ds_read_b64_tr_b16 v[128:129], v248 offset:3584\n" \
    "ds_read_b64_tr_b16 v[130:131], v248 offset:4608\n" \
    "ds_read_b64_tr_b16 v[132:133], v248 offset:5632\n" \
    "ds_read_b64_tr_b16 v[134:135], v248 offset:6656\n" \
    "ds_read_b64_tr_b16 v[136:137], v248 offset:7168\n" \
    "ds_read_b64_tr_b16 v[138:139], v248 offset:8192\n" \
    "ds_read_b64_tr_b16 v[140:141], v248 offset:9216\n" \
    "ds_read_b64_tr_b16 v[142:143], v248 offset:10240\n" \
    "ds_read_b64_tr_b16 v[144:145], v248 offset:10752\n" \
    "ds_read_b64_tr_b16 v[146:147], v248 offset:11776\n" \
    "ds_read_b64_tr_b16 v[148:149], v248 offset:12800\n" \
    "ds_read_b64_tr_b16 v[150:151], v248 offset:13824\n" \
    "ds_read_b64_tr_b16 v[152:153], v248 offset:14336\n" \
    "ds_read_b64_tr_b16 v[154:155], v248 offset:15360\n" \
    "ds_read_b64_tr_b16 v[156:157], v248 offset:16384\n" \
    "ds_read_b64_tr_b16 v[158:159], v248 offset:17408\n" \
    "ds_read_b64_tr_b16 v[160:161], v248 offset:17920\n" \
    "ds_read_b64_tr_b16 v[162:163], v248 offset:18944\n" \
    "ds_read_b64_tr_b16 v[164:165], v248 offset:19968\n" \
    "ds_read_b64_tr_b16 v[166:167], v248 offset:20992\n" \
    "ds_read_b64_tr_b16 v[168:169], v248 offset:21504\n" \
    "ds_read_b64_tr_b16 v[170:171], v248 offset:22528\n" \
    "ds_read_b64_tr_b16 v[172:173], v248 offset:23552\n" \
    "ds_read_b64_tr_b16 v[174:175], v248 offset:24576\n" \
    "ds_read_b64_tr_b16 v[176:177], v248 offset:25088\n" \
    "ds_read_b64_tr_b16 v[178:179], v248 offset:26112\n" \
    "ds_read_b64_tr_b16 v[180:181], v248 offset:27136\n" \
    "ds_read_b64_tr_b16 v[182:183], v248 offset:28160\n" \
    "ds_read_b64_tr_b16 v[184:185], v248 offset:28672\n" \
    "ds_read_b64_tr_b16 v[186:187], v248 offset:29696\n" \
    "ds_read_b64_tr_b16 v[188:189], v248 offset:30720\n" \
    "ds_read_b64_tr_b16 v[190:191], v248 offset:31744\n" \
    "ds_read_b64_tr_b16 v[192:193], v248 offset:32256\n" \
    "ds_read_b64_tr_b16 v[194:195], v248 offset:33280\n" \
    "ds_read_b64_tr_b16 v[196:197], v248 offset:34304\n" \
    "ds_read_b64_tr_b16 v[198:199], v248 offset:35328\n" \
    "ds_read_b64_tr_b16 v[200:201], v248 offset:35840\n" \
    "ds_read_b64_tr_b16 v[202:203], v248 offset:36864\n" \
    "ds_read_b64_tr_b16 v[204:205], v248 offset:37888\n" \
    "ds_read_b64_tr_b16 v[206:207], v248 offset:38912\n" \
    "ds_read_b64_tr_b16 v[208:209], v248 offset:39424\n" \
    "ds_read_b64_tr_b16 v[210:211], v248 offset:40448\n" \
    "ds_read_b64_tr_b16 v[212:213], v248 offset:41472\n" \
    "ds_read_b64_tr_b16 v[214:215], v248 offset:42496\n" \
    "ds_read_b64_tr_b16 v[216:217], v248 offset:43008\n" \
    "ds_read_b64_tr_b16 v[218:219], v248 offset:44032\n" \
    "ds_read_b64_tr_b16 v[220:221], v248 offset:45056\n" \
    "ds_read_b64_tr_b16 v[222:223], v248 offset:46080\n" \
    "ds_read_b64_tr_b16 v[224:225], v248 offset:46592\n" \
    "ds_read_b64_tr_b16 v[226:227], v248 offset:47616\n" \
    "ds_read_b64_tr_b16 v[228:229], v248 offset:48640\n" \
    "ds_read_b64_tr_b16 v[230:231], v248 offset:49664\n" \
    "ds_read_b64_tr_b16 v[232:233], v248 offset:50176\n" \
    "ds_read_b64_tr_b16 v[234:235], v248 offset:51200\n" \
    "ds_read_b64_tr_b16 v[236:237], v248 offset:52224\n" \
    "ds_read_b64_tr_b16 v[238:239], v248 offset:53248\n" \
    "ds_read_b64_tr_b16 v[240:241], v248 offset:53760\n" \
    "ds_read_b64_tr_b16 v[242:243], v248 offset:54784\n" \
    "ds_read_b64_tr_b16 v[244:245], v248 offset:55808\n" \
    "ds_read_b64_tr_b16 v[246:247], v253 offset:56832\n" \
    "MN_WFD_%=:\n" \
    "s_mov_b32 s63, 0x400000\n" \
    "s_lshr_b32 s70, %[wv], 1\n" \
    "s_cmp_eq_u32 %[ct], 3\n" \
    "s_cselect_b32 s70, s70, 0\n" \
    "s_lshl_b32 s70, s70, 4\n" \
    "s_waitcnt lgkmcnt(0)\n" \
    "ds_read_b32 v11, v10\n" \
    "ds_read_b128 v[80:83], v255 offset:0\n" \
    "ds_read_b128 v[84:87], v255 offset:1024\n" \
    "ds_read_b128 v[88:91], v255 offset:2048\n" \
    "ds_read_b128 v[92:95], v255 offset:3072\n" \
    "ds_read_b128 v[96:99], v255 offset:4096\n" \
    "ds_read_b128 v[100:103], v255 offset:5120\n" \
    "ds_read_b128 v[104:107], v255 offset:6144\n" \
    "ds_read_b128 v[108:111], v255 offset:7168\n" \
    "s_waitcnt lgkmcnt(0)\n" \
    "v_readfirstlane_b32 s61, v11\n" \
    "s_sub_u32 s63, s63, 1\n" \
    "s_cbranch_scc1 MN_P3G0_%=\n" \
    "s_lshr_b32 s61, s61, s70\n" \
    "s_bfe_u32 s61, s61, 262144\n" \
    "s_cmp_eq_u32 s61, 15\n" \
    "s_cbranch_scc1 MN_P3G0_%=\n" \
    "MN_P3S0_%=:\n" \
    "ds_read_b32 v11, v10\n" \
    "s_waitcnt lgkmcnt(0)\n" \
    "v_readfirstlane_b32 s61, v11\n" \
    "s_sub_u32 s63, s63, 1\n" \
    "s_cbranch_scc1 MN_P3G0_%=\n" \
    "s_lshr_b32 s61, s61, s70\n" \
    "s_bfe_u32 s61, s61, 262144\n" \
    "s_cmp_eq_u32 s61, 15\n" \
    "s_cbranch_scc0 MN_P3S0_%=\n" \
    "ds_read_b128 v[80:83], v255 offset:0\n" \
    "ds_read_b128 v[84:87], v255 offset:1024\n" \
    "ds_read_b128 v[88:91], v255 offset:2048\n" \
    "ds_read_b128 v[92:95], v255 offset:3072\n" \
    "ds_read_b128 v[96:99], v255 offset:4096\n" \
    "ds_read_b128 v[100:103], v255 offset:5120\n" \
    "ds_read_b128 v[104:107], v255 offset:6144\n" \
    "ds_read_b128 v[108:111], v255 offset:7168\n" \
    "s_waitcnt lgkmcnt(0)\n" \
    "MN_P3G0_%=:\n" \
    "v_mfma_f32_32x32x16_bf16 v[64:79], v[120:123], v[80:83], 0\n" \
    "ds_read_b32 v11, v10\n" \
    "ds_read_b128 v[32:35], v255 offset:8192\n" \
    "v_mfma_f32_32x32x16_bf16 v[64:79], v[124:127], v[84:87], v[64:79]\n" \
    "ds_read_b128 v[36:39], v255 offset:9216\n" \
    "v_mfma_f32_32x32x16_bf16 v[64:79], v[128:131], v[88:91], v[64:79]\n" \
    "ds_read_b128 v[40:43], v255 offset:10240\n" \
    "v_mfma_f32_32x32x16_bf16 v[64:79], v[132:135], v[92:95], v[64:79]\n" \
    "ds_read_b128 v[44:47], v255 offset:11264\n" \
    "v_mfma_f32_32x32x16_bf16 v[64:79], v[136:139], v[96:99], v[64:79]\n" \
    "ds_read_b128 v[48:51], v255 offset:12288\n" \
    "v_mfma_f32_32x32x16_bf16 v[64:79], v[140:143], v[100:103], v[64:79]\n" \
    "ds_read_b128 v[52:55], v255 offset:13312\n" \
    "v_mfma_f32_32x32x16_bf16 v[64:79], v[144:147], v[104:107], v[64:79]\n" \
    "ds_read_b128 v[56:59], v255 offset:14336\n" \
    "v_mfma_f32_32x32x16_bf16 v[64:79], v[148:151], v[108:111], v[64:79]\n" \
    "ds_read_b128 v[60:63], v255 offset:15360\n" \
    "s_waitcnt lgkmcnt(0)\n" \
    "v_readfirstlane_b32 s61, v11\n" \
    "s_sub_u32 s63, s63, 1\n" \
    "s_cbranch_scc1 MN_P3G1_%=\n" \
    "s_lshr_b32 s61, s61, s70\n" \
    "s_bfe_u32 s61, s61, 262148\n" \
    "s_cmp_eq_u32 s61, 15\n" \
    "s_cbranch_scc1 MN_P3G1_%=\n" \
    "MN_P3S1_%=:\n" \
    "ds_read_b32 v11, v10\n" \
    "s_waitcnt lgkmcnt(0)\n" \
    "v_readfirstlane_b32 s61, v11\n" \
    "s_sub_u32 s63, s63, 1\n" \
    "s_cbranch_scc1 MN_P3G1_%=\n" \
    "s_lshr_b32 s61, s61, s70\n" \
    "s_bfe_u32 s61, s61, 262148\n" \
    "s_cmp_eq_u32 s61, 15\n" \
    "s_cbranch_scc0 MN_P3S1_%=\n" \
    "ds_read_b128 v[32:35], v255 offset:8192\n" \
    "ds_read_b128 v[36:39], v255 offset:9216\n" \
    "ds_read_b128 v[40:43], v255 offset:10240\n" \
    "ds_read_b128 v[44:47], v255 offset:11264\n" \
    "ds_read_b128 v[48:51], v255 offset:12288\n" \
    "ds_read_b128 v[52:55], v255 offset:13312\n" \
    "ds_read_b128 v[56:59], v255 offset:14336\n" \
    "ds_read_b128 v[60:63], v255 offset:15360\n" \
    "s_waitcnt lgkmcnt(0)\n" \
    "MN_P3G1_%=:\n" \
    "v_mfma_f32_32x32x16_bf16 v[64:79], v[152:155], v[32:35], v[64:79]\n" \
    "ds_read_b32 v11, v10\n" \
    "ds_read_b128 v[80:83], v255 offset:16384\n" \
    "v_mfma_f32_32x32x16_bf16 v[64:79], v[156:159], v[36:39], v[64:79]\n" \
    "ds_read_b128 v[84:87], v255 offset:17408\n" \
    "v_mfma_f32_32x32x16_bf16 v[64:79], v[160:163], v[40:43], v[64:79]\n" \
    "ds_read_b128 v[88:91], v255 offset:18432\n" \
    "v_mfma_f32_32x32x16_bf16 v[64:79], v[164:167], v[44:47], v[64:79]\n" \
    "ds_read_b128 v[92:95], v255 offset:19456\n" \
    "v_mfma_f32_32x32x16_bf16 v[64:79], v[168:171], v[48:51], v[64:79]\n" \
    "ds_read_b128 v[96:99], v255 offset:20480\n" \
    "v_mfma_f32_32x32x16_bf16 v[64:79], v[172:175], v[52:55], v[64:79]\n" \
    "ds_read_b128 v[100:103], v255 offset:21504\n" \
    "v_mfma_f32_32x32x16_bf16 v[64:79], v[176:179], v[56:59], v[64:79]\n" \
    "ds_read_b128 v[104:107], v255 offset:22528\n" \
    "v_mfma_f32_32x32x16_bf16 v[64:79], v[180:183], v[60:63], v[64:79]\n" \
    "ds_read_b128 v[108:111], v255 offset:23552\n" \
    "s_waitcnt lgkmcnt(0)\n" \
    "v_readfirstlane_b32 s61, v11\n" \
    "s_sub_u32 s63, s63, 1\n" \
    "s_cbranch_scc1 MN_P3G2_%=\n" \
    "s_lshr_b32 s61, s61, s70\n" \
    "s_bfe_u32 s61, s61, 262152\n" \
    "s_cmp_eq_u32 s61, 15\n" \
    "s_cbranch_scc1 MN_P3G2_%=\n" \
    "MN_P3S2_%=:\n" \
    "ds_read_b32 v11, v10\n" \
    "s_waitcnt lgkmcnt(0)\n" \
    "v_readfirstlane_b32 s61, v11\n" \
    "s_sub_u32 s63, s63, 1\n" \
    "s_cbranch_scc1 MN_P3G2_%=\n" \
    "s_lshr_b32 s61, s61, s70\n" \
    "s_bfe_u32 s61, s61, 262152\n" \
    "s_cmp_eq_u32 s61, 15\n" \
    "s_cbranch_scc0 MN_P3S2_%=\n" \
    "ds_read_b128 v[80:83], v255 offset:16384\n" \
    "ds_read_b128 v[84:87], v255 offset:17408\n" \
    "ds_read_b128 v[88:91], v255 offset:18432\n" \
    "ds_read_b128 v[92:95], v255 offset:19456\n" \
    "ds_read_b128 v[96:99], v255 offset:20480\n" \
    "ds_read_b128 v[100:103], v255 offset:21504\n" \
    "ds_read_b128 v[104:107], v255 offset:22528\n" \
    "ds_read_b128 v[108:111], v255 offset:23552\n" \
    "s_waitcnt lgkmcnt(0)\n" \
    "MN_P3G2_%=:\n" \
    "v_mfma_f32_32x32x16_bf16 v[64:79], v[184:187], v[80:83], v[64:79]\n" \
    "ds_read_b32 v11, v10\n" \
    "ds_read_b128 v[32:35], v255 offset:24576\n" \
    "v_mfma_f32_32x32x16_bf16 v[64:79], v[188:191], v[84:87], v[64:79]\n" \
    "ds_read_b128 v[36:39], v255 offset:25600\n" \
    "v_mfma_f32_32x32x16_bf16 v[64:79], v[192:195], v[88:91], v[64:79]\n" \
    "ds_read_b128 v[40:43], v255 offset:26624\n" \
    "v_mfma_f32_32x32x16_bf16 v[64:79], v[196:199], v[92:95], v[64:79]\n" \
    "ds_read_b128 v[44:47], v255 offset:27648\n" \
    "v_mfma_f32_32x32x16_bf16 v[64:79], v[200:203], v[96:99], v[64:79]\n" \
    "v_mfma_f32_32x32x16_bf16 v[64:79], v[204:207], v[100:103], v[64:79]\n" \
    "v_mfma_f32_32x32x16_bf16 v[64:79], v[208:211], v[104:107], v[64:79]\n" \
    "v_mfma_f32_32x32x16_bf16 v[64:79], v[212:215], v[108:111], v[64:79]\n" \
    "s_waitcnt lgkmcnt(0)\n" \
    "v_readfirstlane_b32 s61, v11\n" \
    "s_sub_u32 s63, s63, 1\n" \
    "s_cbranch_scc1 MN_P3G3_%=\n" \
    "s_lshr_b32 s61, s61, s70\n" \
    "s_bfe_u32 s61, s61, 131084\n" \
    "s_cmp_eq_u32 s61, 3\n" \
    "s_cbranch_scc1 MN_P3G3_%=\n" \
    "MN_P3S3_%=:\n" \
    "ds_read_b32 v11, v10\n" \
    "s_waitcnt lgkmcnt(0)\n" \
    "v_readfirstlane_b32 s61, v11\n" \
    "s_sub_u32 s63, s63, 1\n" \
    "s_cbranch_scc1 MN_P3G3_%=\n" \
    "s_lshr_b32 s61, s61, s70\n" \
    "s_bfe_u32 s61, s61, 131084\n" \
    "s_cmp_eq_u32 s61, 3\n" \
    "s_cbranch_scc0 MN_P3S3_%=\n" \
    "ds_read_b128 v[32:35], v255 offset:24576\n" \
    "ds_read_b128 v[36:39], v255 offset:25600\n" \
    "ds_read_b128 v[40:43], v255 offset:26624\n" \
    "ds_read_b128 v[44:47], v255 offset:27648\n" \
    "s_waitcnt lgkmcnt(0)\n" \
    "MN_P3G3_%=:\n" \
    "v_mfma_f32_32x32x16_bf16 v[64:79], v[216:219], v[32:35], v[64:79]\n" \
    "ds_read_b32 v11, v10\n" \
    "ds_read_b128 v[80:83], v255 offset:28672\n" \
    "v_mfma_f32_32x32x16_bf16 v[64:79], v[220:223], v[36:39], v[64:79]\n" \
    "ds_read_b128 v[84:87], v255 offset:29696\n" \
    "v_mfma_f32_32x32x16_bf16 v[64:79], v[224:227], v[40:43], v[64:79]\n" \
    "ds_read_b128 v[88:91], v255 offset:30720\n" \
    "v_mfma_f32_32x32x16_bf16 v[64:79], v[228:231], v[44:47], v[64:79]\n" \
    "ds_read_b128 v[92:95], v255 offset:31744\n" \
    "s_waitcnt lgkmcnt(0)\n" \
    "v_readfirstlane_b32 s61, v11\n" \
    "s_sub_u32 s63, s63, 1\n" \
    "s_cbranch_scc1 MN_P3G4_%=\n" \
    "s_lshr_b32 s61, s61, s70\n" \
    "s_bfe_u32 s61, s61, 131086\n" \
    "s_cmp_eq_u32 s61, 3\n" \
    "s_cbranch_scc1 MN_P3G4_%=\n" \
    "MN_P3S4_%=:\n" \
    "ds_read_b32 v11, v10\n" \
    "s_waitcnt lgkmcnt(0)\n" \
    "v_readfirstlane_b32 s61, v11\n" \
    "s_sub_u32 s63, s63, 1\n" \
    "s_cbranch_scc1 MN_P3G4_%=\n" \
    "s_lshr_b32 s61, s61, s70\n" \
    "s_bfe_u32 s61, s61, 131086\n" \
    "s_cmp_eq_u32 s61, 3\n" \
    "s_cbranch_scc0 MN_P3S4_%=\n" \
    "ds_read_b128 v[80:83], v255 offset:28672\n" \
    "ds_read_b128 v[84:87], v255 offset:29696\n" \
    "ds_read_b128 v[88:91], v255 offset:30720\n" \
    "ds_read_b128 v[92:95], v255 offset:31744\n" \
    "s_waitcnt lgkmcnt(0)\n" \
    "MN_P3G4_%=:\n" \
    "v_mfma_f32_32x32x16_bf16 v[64:79], v[232:235], v[80:83], v[64:79]\n" \
    "v_mfma_f32_32x32x16_bf16 v[64:79], v[236:239], v[84:87], v[64:79]\n" \
    "v_mfma_f32_32x32x16_bf16 v[64:79], v[240:243], v[88:91], v[64:79]\n" \
    "v_mfma_f32_32x32x16_bf16 v[64:79], v[244:247], v[92:95], v[64:79]\n" \
    "s_nop 7\n" \
    "s_nop 3\n" \
    "v_cvt_pk_bf16_f32 v80, v64, v65\n" \
    "v_cvt_pk_bf16_f32 v81, v66, v67\n" \
    "ds_write_b64 v249, v[80:81] offset:0\n" \
    "v_cvt_pk_bf16_f32 v82, v68, v69\n" \
    "v_cvt_pk_bf16_f32 v83, v70, v71\n" \
    "ds_write_b64 v249, v[82:83] offset:16\n" \
    "v_cvt_pk_bf16_f32 v84, v72, v73\n" \
    "v_cvt_pk_bf16_f32 v85, v74, v75\n" \
    "ds_write_b64 v249, v[84:85] offset:32\n" \
    "v_cvt_pk_bf16_f32 v86, v76, v77\n" \
    "v_cvt_pk_bf16_f32 v87, v78, v79\n" \
    "ds_write_b64 v249, v[86:87] offset:48\n" \
    "s_waitcnt lgkmcnt(0)\n" \
    "s_barrier\n" \
    "fk_end_a:\n" \
    "ds_read_b32 v9, v252 offset:256\n" \
    "ds_read_b32 v8, v252\n" \
    "ds_read_b128 v[64:67], v251\n" \
    "s_waitcnt vmcnt(0)\n" \
    "v_and_b32 v73, 0xffff0000, v12\n" \
    "v_lshlrev_b32 v72, 16, v12\n" \
    "v_and_b32 v75, 0xffff0000, v13\n" \
    "v_lshlrev_b32 v74, 16, v13\n" \
    "v_and_b32 v77, 0xffff0000, v14\n" \
    "v_lshlrev_b32 v76, 16, v14\n" \
    "v_and_b32 v79, 0xffff0000, v15\n" \
    "v_lshlrev_b32 v78, 16, v15\n" \
    "s_waitcnt lgkmcnt(2)\n" \
    "v_cmp_le_i32 vcc, 0, v9\n" \
    "s_cbranch_vccz MN_NOY_%=\n" \
    "s_and_saveexec_b64 s[74:75], vcc\n" \
    "v_lshl_add_u32 v3, v9, 9, v254\n" \
    "ds_read_b128 v[80:83], v3\n" \
    "ds_read_b128 v[84:87], v3 offset:16\n" \
    "s_waitcnt lgkmcnt(0)\n" \
    "v_pk_add_f32 v[72:73], v[72:73], v[80:81]\n" \
    "v_pk_add_f32 v[74:75], v[74:75], v[82:83]\n" \
    "v_pk_add_f32 v[76:77], v[76:77], v[84:85]\n" \
    "v_pk_add_f32 v[78:79], v[78:79], v[86:87]\n" \
    "s_mov_b64 exec, s[74:75]\n" \
    "MN_NOY_%=:\n" \
    "s_waitcnt lgkmcnt(0)\n" \
    "v_sub_f32 v10, 1.0, v8\n" \
    "v_and_b32 v81, 0xffff0000, v64\n" \
    "v_lshlrev_b32 v80, 16, v64\n" \
    "v_and_b32 v83, 0xffff0000, v65\n" \
    "v_lshlrev_b32 v82, 16, v65\n" \
    "v_and_b32 v85, 0xffff0000, v66\n" \
    "v_lshlrev_b32 v84, 16, v66\n" \
    "v_and_b32 v87, 0xffff0000, v67\n" \
    "v_lshlrev_b32 v86, 16, v67\n" \
    "v_pk_mul_f32 v[72:73], v[10:11], v[72:73] op_sel_hi:[0,1]\n" \
    "v_pk_mul_f32 v[74:75], v[10:11], v[74:75] op_sel_hi:[0,1]\n" \
    "v_pk_mul_f32 v[76:77], v[10:11], v[76:77] op_sel_hi:[0,1]\n" \
    "v_pk_mul_f32 v[78:79], v[10:11], v[78:79] op_sel_hi:[0,1]\n" \
    "v_pk_fma_f32 v[72:73], v[8:9], v[80:81], v[72:73] op_sel_hi:[0,1,1]\n" \
    "v_pk_fma_f32 v[74:75], v[8:9], v[82:83], v[74:75] op_sel_hi:[0,1,1]\n" \
    "v_pk_fma_f32 v[76:77], v[8:9], v[84:85], v[76:77] op_sel_hi:[0,1,1]\n" \
    "v_pk_fma_f32 v[78:79], v[8:9], v[86:87], v[78:79] op_sel_hi:[0,1,1]\n" \
    "v_cvt_pk_bf16_f32 v64, v72, v73\n" \
    "v_cvt_pk_bf16_f32 v65, v74, v75\n" \
    "v_cvt_pk_bf16_f32 v66, v76, v77\n" \
    "v_cvt_pk_bf16_f32 v67, v78, v79\n" \
    "buffer_store_dwordx4 v[64:67], v250, %[rout], 0 offen sc1\n" \
    "MN_END_%=:\n" \
    "s_nop 1\n" \
    ""
#define MAIN_CLOB "v1", "v2", "v3", "v8", "v9", "v10", "v11", "v13", "v14", "v15", "v20", "v21", "v22", "v23", "v28", "v29", "v30", "v31", "v40", "v41", "v42", "v43", "v44", "v45", "v46", "v47", "v48", "v49", "v50", "v51", "v52", "v53", "v54", "v55", "v56", "v57", "v58", "v59", "v60", "v61", "v62", "v63", "v64", "v65", "v66", "v67", "v68", "v69", "v70", "v71", "v72", "v73", "v74", "v75", "v76", "v77", "v78", "v79", "v80", "v81", "v82", "v83", "v84", "v85", "v86", "v87", "v88", "v89", "v90", "v91", "v92", "v93", "v94", "v95", "v96", "v97", "v98", "v99", "v100", "v101", "v102", "v103", "v104", "v105", "v106", "v107", "v108", "v109", "v110", "v111", "v112", "v113", "v114", "v115", "v116", "v117", "v118", "v119", "v120", "v121", "v122", "v123", "v124", "v125", "v126", "v127", "v128", "v129", "v130", "v131", "v132", "v133", "v134", "v135", "v136", "v137", "v138", "v139", "v140", "v141", "v142", "v143", "v144", "v145", "v146", "v147", "v148", "v149", "v150", "v151", "v152", "v153", "v154", "v155", "v156", "v157", "v158", "v159", "v160", "v161", "v162", "v163", "v164", "v165", "v166", "v167", "v168", "v169", "v170", "v171", "v172", "v173", "v174", "v175", "v176", "v177", "v178", "v179", "v180", "v181", "v182", "v183", "v184", "v185", "v186", "v187", "v188", "v189", "v190", "v191", "v192", "v193", "v194", "v195", "v196", "v197", "v198", "v199", "v200", "v201", "v202", "v203", "v204", "v205", "v206", "v207", "v208", "v209", "v210", "v211", "v212", "v213", "v214", "v215", "v216", "v217", "v218", "v219", "v220", "v221", "v222", "v223", "v224", "v225", "v226", "v227", "v228", "v229", "v230", "v231", "v232", "v233", "v234", "v235", "v236", "v237", "v238", "v239", "v240", "v241", "v242", "v243", "v244", "v245", "v246", "v247", "s60", "s61", "s62", "s63", "s64", "s65", "s66", "s67", "s68", "s69", "s70", "s71", "s72", "s73", "s74", "s75", "s76", "s77", "s78", "s79"

#define ENTRY_ASM \
    "s_load_dwordx4 s[72:75], %[ka], 0x0\n" \
    "s_load_dwordx2 s[64:65], %[ka], 0x18\n" \
    "s_load_dwordx2 s[76:77], %[ka], 0x38\n" \
    "s_load_dwordx2 s[70:71], %[ka], 0x48\n" \
    "v_readfirstlane_b32 s69, v0\n" \
    "v_and_b32 v1, 63, v0\n" \
    "s_cmp_lt_u32 s69, 0x100\n" \
    "s_cbranch_scc0 EN_HELP_%=\n" \
    "s_setprio 3\n" \
    "s_lshr_b32 s69, s69, 6\n" \
    "s_mul_i32 s67, s69, 224\n" \
    "v_lshlrev_b32 v3, 2, v1\n" \
    "v_add_u32 v2, s67, v3\n" \
    "s_getpc_b64 s[60:61]\n" \
    "fk_pf_here:\n" \
    "s_mov_b32 s62, fk_end_a-fk_pf_here\n" \
    "s_max_i32 s62, s62, fk_end_b-fk_pf_here\n" \
    "s_add_u32 s62, s62, 0x100\n" \
    "s_and_b32 s61, s61, 0xffff\n" \
    "s_mov_b32 s63, 0x20000\n" \
    "s_bfe_u32 s66, %[b], 0x40003\n" \
    "s_lshl2_add_u32 s66, s66, s69\n" \
    "s_mul_i32 s66, s66, 640\n" \
    "v_lshlrev_b32 v4, 7, v1\n" \
    "s_mov_b32 m0, 152576\n" \
    "s_mov_b64 exec, 31\n" \
    "buffer_load_dword v4, s[60:63], s66 offen lds\n" \
    "s_mov_b32 m0, 163328\n" \
    "s_mov_b64 exec, 3\n" \
    "s_waitcnt lgkmcnt(0)\n" \
    "global_load_lds_dword v3, s[70:71] sc1\n" \
    "s_add_u32 m0, s67, 152832\n" \
    "s_mov_b32 exec_lo, -1\n" \
    "s_mov_b32 exec_hi, 0xffffff\n" \
    "global_load_lds_dword v2, s[64:65]\n" \
    "s_add_u32 m0, s67, 153728\n" \
    "s_nop 0\n" \
    "global_load_lds_dword v2, s[76:77]\n" \
    "s_mov_b64 exec, -1\n" \
    "s_setprio 0\n" \
    "s_branch EN_DONE_%=\n" \
    "EN_HELP_%=:\n" \
    "s_bfe_u32 s61, %[b], 0x20003\n" \
    "s_lshr_b32 s62, %[b], 5\n" \
    "s_and_b32 s67, %[b], 3\n" \
    "s_lshl_b32 s67, s67, 2\n" \
    "s_or_b32 s67, s67, s61\n" \
    "s_bfe_u32 s68, %[b], 0x10002\n" \
    "s_lshl1_add_u32 s68, s62, s68\n" \
    "s_lshl1_add_u32 s60, s61, s62\n" \
    "s_sub_u32 s63, s60, 7\n" \
    "s_cmp_ge_u32 s60, 7\n" \
    "s_cselect_b32 s60, s63, s60\n" \
    "s_setprio 2\n" \
    "s_bfe_u32 s69, s69, 0x20006\n" \
    "v_lshrrev_b32 v2, 3, v1\n" \
    "v_and_b32 v3, 7, v1\n" \
    "s_lshl_b32 s61, s67, 7\n" \
    "s_lshl_b32 s62, s69, 5\n" \
    "s_add_u32 s61, s61, s62\n" \
    "v_add_u32 v4, s61, v2\n" \
    "v_mul_u32_u24 v4, 0x380, v4\n" \
    "v_lshrrev_b32 v5, 1, v2\n" \
    "v_xor_b32 v6, v3, v5\n" \
    "v_lshl_add_u32 v4, v6, 4, v4\n" \
    "v_xor_b32 v7, 64, v4\n" \
    "v_add_u32 v6, 0x3000, v4\n" \
    "v_and_b32 v5, 1, v5\n" \
    "v_lshlrev_b32 v5, 2, v5\n" \
    "v_xor_b32 v3, v3, v5\n" \
    "v_add_u32 v5, 0x1800, v7\n" \
    "v_add_u32 v7, 0x4800, v7\n" \
    "s_lshl_b32 s61, s68, 7\n" \
    "s_add_u32 s61, s61, 0x380\n" \
    "v_mul_u32_u24 v8, 0xa80, v2\n" \
    "v_lshl_add_u32 v8, v3, 4, v8\n" \
    "v_add_u32 v8, s61, v8\n" \
    "v_add_u32 v9, 0x5000, v8\n" \
    "s_mul_i32 s61, s68, 56\n" \
    "s_and_b32 s61, s61, -16\n" \
    "v_lshrrev_b32 v10, 2, v1\n" \
    "v_mul_u32_u24 v10, 0xa80, v10\n" \
    "v_and_b32 v3, 3, v1\n" \
    "v_lshl_add_u32 v10, v3, 4, v10\n" \
    "v_add_u32 v10, s61, v10\n" \
    "s_mov_b32 s78, 0x126000\n" \
    "s_mov_b32 s79, 0x20000\n" \
    "s_waitcnt lgkmcnt(0)\n" \
    "s_lshl_b32 s70, s69, 14\n" \
    "s_add_u32 s70, s70, 57344\n" \
    "s_mov_b32 s76, s74\n" \
    "s_and_b32 s77, s75, 0xffff\n" \
    "s_and_b32 s73, s73, 0xffff\n" \
    "s_mov_b32 s74, 0x1c0000\n" \
    "s_mov_b32 s75, 0x20000\n" \
    "s_cmp_lt_u32 s68, 14\n" \
    "s_cbranch_scc0 EN_SPARE_%=\n" \
    "s_add_u32 m0, s70, 0\n" \
    "s_lshl_b32 s61, s60, 7\n" \
    "buffer_load_dwordx4 v4, s[72:75], s61 offen offset:0 lds\n" \
    "buffer_load_dwordx4 v5, s[72:75], s61 offen offset:1024 lds\n" \
    "buffer_load_dwordx4 v6, s[72:75], s61 offen offset:2048 lds\n" \
    "buffer_load_dwordx4 v7, s[72:75], s61 offen offset:3072 lds\n" \
    "s_lshl_b32 s61, s60, 2\n" \
    "s_add_u32 s61, s61, s69\n" \
    "s_lshl_b32 m0, s61, 11\n" \
    "s_mul_i32 s61, s61, 43008\n" \
    "buffer_load_dwordx4 v8, s[76:79], s61 offen lds\n" \
    "buffer_load_dwordx4 v9, s[76:79], s61 offen offset:1024 lds\n" \
    "s_add_u32 s60, s60, 1\n" \
    "s_cmp_eq_u32 s60, 7\n" \
    "s_cselect_b32 s60, 0, s60\n" \
    "s_add_u32 m0, s70, 4096\n" \
    "s_lshl_b32 s61, s60, 7\n" \
    "buffer_load_dwordx4 v4, s[72:75], s61 offen offset:0 lds\n" \
    "buffer_load_dwordx4 v5, s[72:75], s61 offen offset:1024 lds\n" \
    "buffer_load_dwordx4 v6, s[72:75], s61 offen offset:2048 lds\n" \
    "buffer_load_dwordx4 v7, s[72:75], s61 offen offset:3072 lds\n" \
    "s_lshl_b32 s61, s60, 2\n" \
    "s_add_u32 s61, s61, s69\n" \
    "s_lshl_b32 m0, s61, 11\n" \
    "s_mul_i32 s61, s61, 43008\n" \
    "buffer_load_dwordx4 v8, s[76:79], s61 offen lds\n" \
    "buffer_load_dwordx4 v9, s[76:79], s61 offen offset:1024 lds\n" \
    "s_add_u32 s60, s60, 1\n" \
    "s_cmp_eq_u32 s60, 7\n" \
    "s_cselect_b32 s60, 0, s60\n" \
    "s_branch EN_DONE_%=\n" \
    "EN_SPARE_%=:\n" \
    "s_add_u32 m0, s70, 0\n" \
    "s_lshl_b32 s61, s60, 7\n" \
    "buffer_load_dwordx4 v4, s[72:75], s61 offen offset:0 lds\n" \
    "buffer_load_dwordx4 v5, s[72:75], s61 offen offset:1024 lds\n" \
    "buffer_load_dwordx4 v6, s[72:75], s61 offen offset:2048 lds\n" \
    "buffer_load_dwordx4 v7, s[72:75], s61 offen offset:3072 lds\n" \
    "s_lshl_b32 s61, s60, 2\n" \
    "s_add_u32 s61, s61, s69\n" \
    "s_lshl_b32 s62, s61, 10\n" \
    "s_add_u32 m0, s62, 122880\n" \
    "s_mul_i32 s61, s61, 43008\n" \
    "buffer_load_dwordx4 v10, s[76:79], s61 offen lds\n" \
    "s_add_u32 s60, s60, 1\n" \
    "s_cmp_eq_u32 s60, 7\n" \
    "s_cselect_b32 s60, 0, s60\n" \
    "s_add_u32 m0, s70, 4096\n" \
    "s_lshl_b32 s61, s60, 7\n" \
    "buffer_load_dwordx4 v4, s[72:75], s61 offen offset:0 lds\n" \
    "buffer_load_dwordx4 v5, s[72:75], s61 offen offset:1024 lds\n" \
    "buffer_load_dwordx4 v6, s[72:75], s61 offen offset:2048 lds\n" \
    "buffer_load_dwordx4 v7, s[72:75], s61 offen offset:3072 lds\n" \
    "s_lshl_b32 s61, s60, 2\n" \
    "s_add_u32 s61, s61, s69\n" \
    "s_lshl_b32 s62, s61, 10\n" \
    "s_add_u32 m0, s62, 122880\n" \
    "s_mul_i32 s61, s61, 43008\n" \
    "buffer_load_dwordx4 v10, s[76:79], s61 offen lds\n" \
    "s_add_u32 s60, s60, 1\n" \
    "s_cmp_eq_u32 s60, 7\n" \
    "s_cselect_b32 s60, 0, s60\n" \
    "EN_DONE_%=:\n" \
    "s_waitcnt lgkmcnt(0)\n" \
    ""
#define LOAD_ASM \
    "s_add_u32 s60, %[rot], 2\n" \
    "s_sub_u32 s61, s60, 7\n" \
    "s_cmp_ge_u32 s60, 7\n" \
    "s_cselect_b32 s60, s61, s60\n" \
    "s_cmp_lt_u32 %[idx], 14\n" \
    "s_cbranch_scc0 LD_SPARE_%=\n" \
    "s_add_u32 m0, %[xr], 8192\n" \
    "s_lshl_b32 s61, s60, 7\n" \
    "buffer_load_dwordx4 v4, %[rx], s61 offen offset:0 lds\n" \
    "buffer_load_dwordx4 v5, %[rx], s61 offen offset:1024 lds\n" \
    "buffer_load_dwordx4 v6, %[rx], s61 offen offset:2048 lds\n" \
    "buffer_load_dwordx4 v7, %[rx], s61 offen offset:3072 lds\n" \
    "s_lshl_b32 s61, s60, 2\n" \
    "s_add_u32 s61, s61, %[hw]\n" \
    "s_lshl_b32 m0, s61, 11\n" \
    "s_mul_i32 s61, s61, 43008\n" \
    "buffer_load_dwordx4 v8, %[rw], s61 offen lds\n" \
    "buffer_load_dwordx4 v9, %[rw], s61 offen offset:1024 lds\n" \
    "s_add_u32 s60, s60, 1\n" \
    "s_cmp_eq_u32 s60, 7\n" \
    "s_cselect_b32 s60, 0, s60\n" \
    "s_waitcnt vmcnt(12)\n" \
    "s_barrier\n" \
    "s_add_u32 m0, %[xr], 12288\n" \
    "s_lshl_b32 s61, s60, 7\n" \
    "buffer_load_dwordx4 v4, %[rx], s61 offen offset:0 lds\n" \
    "buffer_load_dwordx4 v5, %[rx], s61 offen offset:1024 lds\n" \
    "buffer_load_dwordx4 v6, %[rx], s61 offen offset:2048 lds\n" \
    "buffer_load_dwordx4 v7, %[rx], s61 offen offset:3072 lds\n" \
    "s_lshl_b32 s61, s60, 2\n" \
    "s_add_u32 s61, s61, %[hw]\n" \
    "s_lshl_b32 m0, s61, 11\n" \
    "s_mul_i32 s61, s61, 43008\n" \
    "buffer_load_dwordx4 v8, %[rw], s61 offen lds\n" \
    "buffer_load_dwordx4 v9, %[rw], s61 offen offset:1024 lds\n" \
    "s_add_u32 s60, s60, 1\n" \
    "s_cmp_eq_u32 s60, 7\n" \
    "s_cselect_b32 s60, 0, s60\n" \
    "s_waitcnt vmcnt(12)\n" \
    "s_barrier\n" \
    "s_add_u32 m0, %[xr], 0\n" \
    "s_lshl_b32 s61, s60, 7\n" \
    "buffer_load_dwordx4 v4, %[rx], s61 offen offset:0 lds\n" \
    "buffer_load_dwordx4 v5, %[rx], s61 offen offset:1024 lds\n" \
    "buffer_load_dwordx4 v6, %[rx], s61 offen offset:2048 lds\n" \
    "buffer_load_dwordx4 v7, %[rx], s61 offen offset:3072 lds\n" \
    "s_lshl_b32 s61, s60, 2\n" \
    "s_add_u32 s61, s61, %[hw]\n" \
    "s_lshl_b32 m0, s61, 11\n" \
    "s_mul_i32 s61, s61, 43008\n" \
    "buffer_load_dwordx4 v8, %[rw], s61 offen lds\n" \
    "buffer_load_dwordx4 v9, %[rw], s61 offen offset:1024 lds\n" \
    "s_add_u32 s60, s60, 1\n" \
    "s_cmp_eq_u32 s60, 7\n" \
    "s_cselect_b32 s60, 0, s60\n" \
    "s_waitcnt vmcnt(12)\n" \
    "s_barrier\n" \
    "s_add_u32 m0, %[xr], 4096\n" \
    "s_lshl_b32 s61, s60, 7\n" \
    "buffer_load_dwordx4 v4, %[rx], s61 offen offset:0 lds\n" \
    "buffer_load_dwordx4 v5, %[rx], s61 offen offset:1024 lds\n" \
    "buffer_load_dwordx4 v6, %[rx], s61 offen offset:2048 lds\n" \
    "buffer_load_dwordx4 v7, %[rx], s61 offen offset:3072 lds\n" \
    "s_lshl_b32 s61, s60, 2\n" \
    "s_add_u32 s61, s61, %[hw]\n" \
    "s_lshl_b32 m0, s61, 11\n" \
    "s_mul_i32 s61, s61, 43008\n" \
    "buffer_load_dwordx4 v8, %[rw], s61 offen lds\n" \
    "buffer_load_dwordx4 v9, %[rw], s61 offen offset:1024 lds\n" \
    "s_add_u32 s60, s60, 1\n" \
    "s_cmp_eq_u32 s60, 7\n" \
    "s_cselect_b32 s60, 0, s60\n" \
    "s_waitcnt vmcnt(12)\n" \
    "s_barrier\n" \
    "s_add_u32 m0, %[xr], 8192\n" \
    "s_lshl_b32 s61, s60, 7\n" \
    "buffer_load_dwordx4 v4, %[rx], s61 offen offset:0 lds\n" \
    "buffer_load_dwordx4 v5, %[rx], s61 offen offset:1024 lds\n" \
    "buffer_load_dwordx4 v6, %[rx], s61 offen offset:2048 lds\n" \
    "buffer_load_dwordx4 v7, %[rx], s61 offen offset:3072 lds\n" \
    "s_lshl_b32 s61, s60, 2\n" \
    "s_add_u32 s61, s61, %[hw]\n" \
    "s_lshl_b32 m0, s61, 11\n" \
    "s_mul_i32 s61, s61, 43008\n" \
    "buffer_load_dwordx4 v8, %[rw], s61 offen lds\n" \
    "buffer_load_dwordx4 v9, %[rw], s61 offen offset:1024 lds\n" \
    "s_add_u32 s60, s60, 1\n" \
    "s_cmp_eq_u32 s60, 7\n" \
    "s_cselect_b32 s60, 0, s60\n" \
    "s_waitcnt vmcnt(12)\n" \
    "s_barrier\n" \
    "v_mov_b32 v29, 163328\n" \
    "ds_read_b64 v[22:23], v29\n" \
    "ds_read_u16 v24, v11\n" \
    "ds_read_u16 v25, v11 offset:896\n" \
    "ds_read_u16 v26, v12\n" \
    "ds_read_u16 v27, v12 offset:896\n" \
    "s_waitcnt lgkmcnt(0)\n" \
    "v_readfirstlane_b32 s64, v22\n" \
    "v_readfirstlane_b32 s65, v23\n" \
    "v_lshl_or_b32 v24, v25, 16, v24\n" \
    "v_lshl_or_b32 v26, v27, 16, v26\n" \
    "v_mov_b32 v25, 0\n" \
    "v_mov_b32 v27, 0\n" \
    "s_add_u32 s64, s64, 1\n" \
    "s_addc_u32 s65, s65, 0\n" \
    "s_mov_b64 %[tag], s[64:65]\n" \
    "s_lshl_b32 s61, s60, 2\n" \
    "s_add_u32 s61, s61, %[hw]\n" \
    "s_lshl_b32 s62, s61, 10\n" \
    "s_add_u32 m0, s62, 122880\n" \
    "s_mul_i32 s61, s61, 43008\n" \
    "buffer_load_dwordx4 v10, %[rw], s61 offen lds\n" \
    "s_add_u32 s60, s60, 1\n" \
    "s_cmp_eq_u32 s60, 7\n" \
    "s_cselect_b32 s60, 0, s60\n" \
    "s_lshl_b32 s61, s60, 2\n" \
    "s_add_u32 s61, s61, %[hw]\n" \
    "s_lshl_b32 s62, s61, 10\n" \
    "s_add_u32 m0, s62, 122880\n" \
    "s_mul_i32 s61, s61, 43008\n" \
    "buffer_load_dwordx4 v10, %[rw], s61 offen lds\n" \
    "s_add_u32 s60, s60, 1\n" \
    "s_cmp_eq_u32 s60, 7\n" \
    "s_cselect_b32 s60, 0, s60\n" \
    "s_lshl_b32 s61, s60, 2\n" \
    "s_add_u32 s61, s61, %[hw]\n" \
    "s_lshl_b32 s62, s61, 10\n" \
    "s_add_u32 m0, s62, 122880\n" \
    "s_mul_i32 s61, s61, 43008\n" \
    "buffer_load_dwordx4 v10, %[rw], s61 offen lds\n" \
    "s_add_u32 s60, s60, 1\n" \
    "s_cmp_eq_u32 s60, 7\n" \
    "s_cselect_b32 s60, 0, s60\n" \
    "s_lshl_b32 s61, s60, 2\n" \
    "s_add_u32 s61, s61, %[hw]\n" \
    "s_lshl_b32 s62, s61, 10\n" \
    "s_add_u32 m0, s62, 122880\n" \
    "s_mul_i32 s61, s61, 43008\n" \
    "buffer_load_dwordx4 v10, %[rw], s61 offen lds\n" \
    "s_add_u32 s60, s60, 1\n" \
    "s_cmp_eq_u32 s60, 7\n" \
    "s_cselect_b32 s60, 0, s60\n" \
    "s_waitcnt vmcnt(10)\n" \
    "s_barrier\n" \
    "s_waitcnt vmcnt(4)\n" \
    "s_barrier\n" \
    "s_barrier\n" \
    "s_lshl_b32 s61, s60, 2\n" \
    "s_add_u32 s61, s61, %[hw]\n" \
    "s_lshl_b32 s62, s61, 10\n" \
    "s_add_u32 m0, s62, 122880\n" \
    "s_mul_i32 s61, s61, 43008\n" \
    "buffer_load_dwordx4 v10, %[rw], s61 offen lds\n" \
    "s_add_u32 s60, s60, 1\n" \
    "s_cmp_eq_u32 s60, 7\n" \
    "s_cselect_b32 s60, 0, s60\n" \
    "s_lshl_b32 s61, s60, 2\n" \
    "s_add_u32 s61, s61, %[hw]\n" \
    "s_lshl_b32 s62, s61, 10\n" \
    "s_add_u32 m0, s62, 122880\n" \
    "s_mul_i32 s61, s61, 43008\n" \
    "buffer_load_dwordx4 v10, %[rw], s61 offen lds\n" \
    "s_add_u32 s60, s60, 1\n" \
    "s_cmp_eq_u32 s60, 7\n" \
    "s_cselect_b32 s60, 0, s60\n" \
    "s_lshl_b32 s61, s60, 2\n" \
    "s_add_u32 s61, s61, %[hw]\n" \
    "s_lshl_b32 s62, s61, 10\n" \
    "s_add_u32 m0, s62, 122880\n" \
    "s_mul_i32 s61, s61, 43008\n" \
    "buffer_load_dwordx4 v10, %[rw], s61 offen lds\n" \
    "s_add_u32 s60, s60, 1\n" \
    "s_cmp_eq_u32 s60, 7\n" \
    "s_cselect_b32 s60, 0, s60\n" \
    "s_waitcnt vmcnt(3)\n" \
    "ds_write_b64 v13, v[24:25]\n" \
    "s_waitcnt lgkmcnt(0)\n" \
    "s_barrier\n" \
    "s_waitcnt vmcnt(0)\n" \
    "s_mov_b32 exec_hi, 0xffff\n" \
    "ds_write_b64 v14, v[26:27]\n" \
    "s_mov_b32 exec_hi, -1\n" \
    "s_waitcnt lgkmcnt(0)\n" \
    "s_barrier\n" \
    "s_branch LD_DONE_%=\n" \
    "LD_SPARE_%=:\n" \
    "s_mul_i32 s63, %[hw], 14336\n" \
    "s_add_u32 m0, %[xr], 8192\n" \
    "s_lshl_b32 s61, s60, 7\n" \
    "buffer_load_dwordx4 v4, %[rx], s61 offen offset:0 lds\n" \
    "buffer_load_dwordx4 v5, %[rx], s61 offen offset:1024 lds\n" \
    "buffer_load_dwordx4 v6, %[rx], s61 offen offset:2048 lds\n" \
    "buffer_load_dwordx4 v7, %[rx], s61 offen offset:3072 lds\n" \
    "s_lshl_b32 s61, s60, 2\n" \
    "s_add_u32 s61, s61, %[hw]\n" \
    "s_lshl_b32 s62, s61, 10\n" \
    "s_add_u32 m0, s62, 122880\n" \
    "s_mul_i32 s61, s61, 43008\n" \
    "buffer_load_dwordx4 v10, %[rw], s61 offen lds\n" \
    "s_add_u32 s60, s60, 1\n" \
    "s_cmp_eq_u32 s60, 7\n" \
    "s_cselect_b32 s60, 0, s60\n" \
    "s_waitcnt vmcnt(10)\n" \
    "s_barrier\n" \
    "s_add_u32 m0, s63, 0\n" \
    "s_mov_b32 s61, 0\n" \
    "buffer_load_dwordx4 v21, %[rg], s61 offen lds\n" \
    "buffer_load_dwordx4 v30, %[rg], s61 offen offset:1024 lds\n" \
    "s_add_u32 m0, s63, 2048\n" \
    "s_mov_b32 s61, 57344\n" \
    "buffer_load_dwordx4 v21, %[rg], s61 offen lds\n" \
    "buffer_load_dwordx4 v30, %[rg], s61 offen offset:1024 lds\n" \
    "s_add_u32 m0, s63, 4096\n" \
    "s_mov_b32 s61, 114688\n" \
    "buffer_load_dwordx4 v21, %[rg], s61 offen lds\n" \
    "buffer_load_dwordx4 v30, %[rg], s61 offen offset:1024 lds\n" \
    "s_add_u32 m0, s63, 6144\n" \
    "s_mov_b32 s61, 172032\n" \
    "buffer_load_dwordx4 v21, %[rg], s61 offen lds\n" \
    "buffer_load_dwordx4 v30, %[rg], s61 offen offset:1024 lds\n" \
    "s_add_u32 m0, %[xr], 12288\n" \
    "s_lshl_b32 s61, s60, 7\n" \
    "buffer_load_dwordx4 v4, %[rx], s61 offen offset:0 lds\n" \
    "buffer_load_dwordx4 v5, %[rx], s61 offen offset:1024 lds\n" \
    "buffer_load_dwordx4 v6, %[rx], s61 offen offset:2048 lds\n" \
    "buffer_load_dwordx4 v7, %[rx], s61 offen offset:3072 lds\n" \
    "s_lshl_b32 s61, s60, 2\n" \
    "s_add_u32 s61, s61, %[hw]\n" \
    "s_lshl_b32 s62, s61, 10\n" \
    "s_add_u32 m0, s62, 122880\n" \
    "s_mul_i32 s61, s61, 43008\n" \
    "buffer_load_dwordx4 v10, %[rw], s61 offen lds\n" \
    "s_add_u32 s60, s60, 1\n" \
    "s_cmp_eq_u32 s60, 7\n" \
    "s_cselect_b32 s60, 0, s60\n" \
    "s_waitcnt vmcnt(18)\n" \
    "s_barrier\n" \
    "s_add_u32 m0, %[xr], 0\n" \
    "s_lshl_b32 s61, s60, 7\n" \
    "buffer_load_dwordx4 v4, %[rx], s61 offen offset:0 lds\n" \
    "buffer_load_dwordx4 v5, %[rx], s61 offen offset:1024 lds\n" \
    "buffer_load_dwordx4 v6, %[rx], s61 offen offset:2048 lds\n" \
    "buffer_load_dwordx4 v7, %[rx], s61 offen offset:3072 lds\n" \
    "s_lshl_b32 s61, s60, 2\n" \
    "s_add_u32 s61, s61, %[hw]\n" \
    "s_lshl_b32 s62, s61, 10\n" \
    "s_add_u32 m0, s62, 122880\n" \
    "s_mul_i32 s61, s61, 43008\n" \
    "buffer_load_dwordx4 v10, %[rw], s61 offen lds\n" \
    "s_add_u32 s60, s60, 1\n" \
    "s_cmp_eq_u32 s60, 7\n" \
    "s_cselect_b32 s60, 0, s60\n" \
    "s_waitcnt vmcnt(18)\n" \
    "s_barrier\n" \
    "s_add_u32 m0, %[xr], 4096\n" \
    "s_lshl_b32 s61, s60, 7\n" \
    "buffer_load_dwordx4 v4, %[rx], s61 offen offset:0 lds\n" \
    "buffer_load_dwordx4 v5, %[rx], s61 offen offset:1024 lds\n" \
    "buffer_load_dwordx4 v6, %[rx], s61 offen offset:2048 lds\n" \
    "buffer_load_dwordx4 v7, %[rx], s61 offen offset:3072 lds\n" \
    "s_lshl_b32 s61, s60, 2\n" \
    "s_add_u32 s61, s61, %[hw]\n" \
    "s_lshl_b32 s62, s61, 10\n" \
    "s_add_u32 m0, s62, 122880\n" \
    "s_mul_i32 s61, s61, 43008\n" \
    "buffer_load_dwordx4 v10, %[rw], s61 offen lds\n" \
    "s_add_u32 s60, s60, 1\n" \
    "s_cmp_eq_u32 s60, 7\n" \
    "s_cselect_b32 s60, 0, s60\n" \
    "s_waitcnt vmcnt(10)\n" \
    "s_barrier\n" \
    "s_add_u32 m0, %[xr], 8192\n" \
    "s_lshl_b32 s61, s60, 7\n" \
    "buffer_load_dwordx4 v4, %[rx], s61 offen offset:0 lds\n" \
    "buffer_load_dwordx4 v5, %[rx], s61 offen offset:1024 lds\n" \
    "buffer_load_dwordx4 v6, %[rx], s61 offen offset:2048 lds\n" \
    "buffer_load_dwordx4 v7, %[rx], s61 offen offset:3072 lds\n" \
    "s_lshl_b32 s61, s60, 2\n" \
    "s_add_u32 s61, s61, %[hw]\n" \
    "s_lshl_b32 s62, s61, 10\n" \
    "s_add_u32 m0, s62, 122880\n" \
    "s_mul_i32 s61, s61, 43008\n" \
    "buffer_load_dwordx4 v10, %[rw], s61 offen lds\n" \
    "s_add_u32 s60, s60, 1\n" \
    "s_cmp_eq_u32 s60, 7\n" \
    "s_cselect_b32 s60, 0, s60\n" \
    "s_waitcnt vmcnt(10)\n" \
    "s_barrier\n" \
    "v_mov_b32 v29, 163328\n" \
    "ds_read_b64 v[22:23], v29\n" \
    "ds_read_u16 v24, v11\n" \
    "ds_read_u16 v25, v11 offset:896\n" \
    "ds_read_u16 v26, v12\n" \
    "ds_read_u16 v27, v12 offset:896\n" \
    "s_waitcnt lgkmcnt(0)\n" \
    "v_readfirstlane_b32 s64, v22\n" \
    "v_readfirstlane_b32 s65, v23\n" \
    "v_lshl_or_b32 v24, v25, 16, v24\n" \
    "v_lshl_or_b32 v26, v27, 16, v26\n" \
    "v_mov_b32 v25, 0\n" \
    "v_mov_b32 v27, 0\n" \
    "s_add_u32 s64, s64, 1\n" \
    "s_addc_u32 s65, s65, 0\n" \
    "s_mov_b64 %[tag], s[64:65]\n" \
    "s_add_u32 m0, s63, 8192\n" \
    "s_mov_b32 s61, 229376\n" \
    "buffer_load_dwordx4 v21, %[rg], s61 offen lds\n" \
    "buffer_load_dwordx4 v30, %[rg], s61 offen offset:1024 lds\n" \
    "s_add_u32 m0, s63, 10240\n" \
    "s_mov_b32 s61, 286720\n" \
    "buffer_load_dwordx4 v21, %[rg], s61 offen lds\n" \
    "buffer_load_dwordx4 v30, %[rg], s61 offen offset:1024 lds\n" \
    "s_waitcnt vmcnt(9)\n" \
    "s_barrier\n" \
    "s_waitcnt vmcnt(4)\n" \
    "s_barrier\n" \
    "ds_write_b64 v13, v[24:25]\n" \
    "s_mov_b32 exec_hi, 0xffff\n" \
    "ds_write_b64 v14, v[26:27]\n" \
    "s_mov_b32 exec_hi, -1\n" \
    "s_waitcnt lgkmcnt(0)\n" \
    "s_barrier\n" \
    "s_barrier\n" \
    "s_barrier\n" \
    "LD_DONE_%=:\n" \
    ""
#define LOAD_CLOB "v22", "v23", "v24", "v25", "v26", "v27", "v28", "v29", "s60", "s61", "s62", "s63", "s64", "s65"

#define MOE_G1_ASM \
    "ds_read_b64_tr_b16 v[64:65], v8 offset:0\n" \
    "ds_read_b64_tr_b16 v[66:67], v8 offset:512\n" \
    "ds_read_b64_tr_b16 v[68:69], v9 offset:0\n" \
    "ds_read_b64_tr_b16 v[70:71], v9 offset:512\n" \
    "ds_read_b64_tr_b16 v[72:73], v10 offset:0\n" \
    "ds_read_b64_tr_b16 v[74:75], v10 offset:512\n" \
    "ds_read_b64_tr_b16 v[76:77], v11 offset:0\n" \
    "ds_read_b64_tr_b16 v[78:79], v11 offset:512\n" \
    "ds_read_b64_tr_b16 v[80:81], v8 offset:4096\n" \
    "ds_read_b64_tr_b16 v[82:83], v8 offset:4608\n" \
    "ds_read_b64_tr_b16 v[84:85], v9 offset:4096\n" \
    "ds_read_b64_tr_b16 v[86:87], v9 offset:4608\n" \
    "ds_read_b64_tr_b16 v[88:89], v10 offset:4096\n" \
    "ds_read_b64_tr_b16 v[90:91], v10 offset:4608\n" \
    "ds_read_b64_tr_b16 v[92:93], v11 offset:4096\n" \
    "ds_read_b64_tr_b16 v[94:95], v11 offset:4608\n" \
    "ds_read_b64_tr_b16 v[96:97], v8 offset:8192\n" \
    "ds_read_b64_tr_b16 v[98:99], v8 offset:8704\n" \
    "ds_read_b64_tr_b16 v[100:101], v9 offset:8192\n" \
    "ds_read_b64_tr_b16 v[102:103], v9 offset:8704\n" \
    "ds_read_b64_tr_b16 v[104:105], v10 offset:8192\n" \
    "ds_read_b64_tr_b16 v[106:107], v10 offset:8704\n" \
    "ds_read_b64_tr_b16 v[108:109], v11 offset:8192\n" \
    "ds_read_b64_tr_b16 v[110:111], v11 offset:8704\n" \
    "s_waitcnt lgkmcnt(0)\n" \
    ""
#define MOE_G2_ASM \
    "ds_read_b64_tr_b16 v[112:113], v12 offset:0\n" \
    "ds_read_b64_tr_b16 v[114:115], v12 offset:512\n" \
    "ds_read_b64_tr_b16 v[116:117], v13 offset:0\n" \
    "ds_read_b64_tr_b16 v[118:119], v13 offset:512\n" \
    "ds_read_b64_tr_b16 v[120:121], v14 offset:0\n" \
    "ds_read_b64_tr_b16 v[122:123], v14 offset:512\n" \
    "ds_read_b64_tr_b16 v[124:125], v15 offset:0\n" \
    "ds_read_b64_tr_b16 v[126:127], v15 offset:512\n" \
    "ds_read_b64_tr_b16 v[128:129], v12 offset:4096\n" \
    "ds_read_b64_tr_b16 v[130:131], v12 offset:4608\n" \
    "ds_read_b64_tr_b16 v[132:133], v13 offset:4096\n" \
    "ds_read_b64_tr_b16 v[134:135], v13 offset:4608\n" \
    "ds_read_b64_tr_b16 v[136:137], v14 offset:4096\n" \
    "ds_read_b64_tr_b16 v[138:139], v14 offset:4608\n" \
    "ds_read_b64_tr_b16 v[140:141], v15 offset:4096\n" \
    "ds_read_b64_tr_b16 v[142:143], v15 offset:4608\n" \
    "s_waitcnt lgkmcnt(0)\n" \
    ""
#define MOE_FIN_ASM \
    "v_mov_b32 v23, 163328\n" \
    "s_mov_b32 s60, 0x400000\n" \
    "s_barrier\n" \
    "ds_read_b128 v[32:35], v31 offset:0\n" \
    "ds_read_b128 v[36:39], v31 offset:64\n" \
    "ds_read_b128 v[40:43], v31 offset:128\n" \
    "ds_read_b128 v[44:47], v31 offset:192\n" \
    "ds_read_b128 v[48:51], v31 offset:256\n" \
    "ds_read_b128 v[52:55], v31 offset:320\n" \
    "ds_read_b128 v[56:59], v31 offset:384\n" \
    "ds_read_b64_tr_b16 v[144:145], v12 offset:0\n" \
    "ds_read_b64_tr_b16 v[146:147], v12 offset:512\n" \
    "ds_read_b64_tr_b16 v[148:149], v13 offset:0\n" \
    "ds_read_b64_tr_b16 v[150:151], v13 offset:512\n" \
    "ds_read_b64_tr_b16 v[152:153], v14 offset:0\n" \
    "ds_read_b64_tr_b16 v[154:155], v14 offset:512\n" \
    "ds_read_b64_tr_b16 v[156:157], v15 offset:0\n" \
    "ds_read_b64_tr_b16 v[158:159], v15 offset:512\n" \
    "s_waitcnt lgkmcnt(14)\n" \
    "v_mfma_f32_16x16x32_bf16 v[176:179], v[64:67], v[32:35], 0\n" \
    "v_mfma_f32_16x16x32_bf16 v[180:183], v[68:71], v[32:35], 0\n" \
    "v_mfma_f32_16x16x32_bf16 v[184:187], v[72:75], v[32:35], 0\n" \
    "v_mfma_f32_16x16x32_bf16 v[188:191], v[76:79], v[32:35], 0\n" \
    "s_waitcnt lgkmcnt(8)\n" \
    "ds_read_b64_tr_b16 v[160:161], v12 offset:4096\n" \
    "ds_read_b64_tr_b16 v[162:163], v12 offset:4608\n" \
    "ds_read_b64_tr_b16 v[164:165], v13 offset:4096\n" \
    "ds_read_b64_tr_b16 v[166:167], v13 offset:4608\n" \
    "ds_read_b64_tr_b16 v[168:169], v14 offset:4096\n" \
    "ds_read_b64_tr_b16 v[170:171], v14 offset:4608\n" \
    "ds_read_b64_tr_b16 v[172:173], v15 offset:4096\n" \
    "ds_read_b64_tr_b16 v[174:175], v15 offset:4608\n" \
    "v_mfma_f32_16x16x32_bf16 v[176:179], v[80:83], v[36:39], v[176:179]\n" \
    "v_mfma_f32_16x16x32_bf16 v[180:183], v[84:87], v[36:39], v[180:183]\n" \
    "v_mfma_f32_16x16x32_bf16 v[184:187], v[88:91], v[36:39], v[184:187]\n" \
    "v_mfma_f32_16x16x32_bf16 v[188:191], v[92:95], v[36:39], v[188:191]\n" \
    "v_mfma_f32_16x16x32_bf16 v[176:179], v[96:99], v[40:43], v[176:179]\n" \
    "v_mfma_f32_16x16x32_bf16 v[180:183], v[100:103], v[40:43], v[180:183]\n" \
    "v_mfma_f32_16x16x32_bf16 v[184:187], v[104:107], v[40:43], v[184:187]\n" \
    "v_mfma_f32_16x16x32_bf16 v[188:191], v[108:111], v[40:43], v[188:191]\n" \
    "v_mfma_f32_16x16x32_bf16 v[176:179], v[112:115], v[44:47], v[176:179]\n" \
    "v_mfma_f32_16x16x32_bf16 v[180:183], v[116:119], v[44:47], v[180:183]\n" \
    "v_mfma_f32_16x16x32_bf16 v[184:187], v[120:123], v[44:47], v[184:187]\n" \
    "v_mfma_f32_16x16x32_bf16 v[188:191], v[124:127], v[44:47], v[188:191]\n" \
    "v_mfma_f32_16x16x32_bf16 v[176:179], v[128:131], v[48:51], v[176:179]\n" \
    "v_mfma_f32_16x16x32_bf16 v[180:183], v[132:135], v[48:51], v[180:183]\n" \
    "v_mfma_f32_16x16x32_bf16 v[184:187], v[136:139], v[48:51], v[184:187]\n" \
    "v_mfma_f32_16x16x32_bf16 v[188:191], v[140:143], v[48:51], v[188:191]\n" \
    "s_waitcnt lgkmcnt(0)\n" \
    "v_mfma_f32_16x16x32_bf16 v[176:179], v[144:147], v[52:55], v[176:179]\n" \
    "v_mfma_f32_16x16x32_bf16 v[180:183], v[148:151], v[52:55], v[180:183]\n" \
    "v_mfma_f32_16x16x32_bf16 v[184:187], v[152:155], v[52:55], v[184:187]\n" \
    "v_mfma_f32_16x16x32_bf16 v[188:191], v[156:159], v[52:55], v[188:191]\n" \
    "v_mfma_f32_16x16x32_bf16 v[176:179], v[160:163], v[56:59], v[176:179]\n" \
    "v_mfma_f32_16x16x32_bf16 v[180:183], v[164:167], v[56:59], v[180:183]\n" \
    "v_mfma_f32_16x16x32_bf16 v[184:187], v[168:171], v[56:59], v[184:187]\n" \
    "v_mfma_f32_16x16x32_bf16 v[188:191], v[172:175], v[56:59], v[188:191]\n" \
    "v_xor_b32 v24, 32, v20\n" \
    "v_xor_b32 v25, 64, v20\n" \
    "v_xor_b32 v26, 96, v20\n" \
    "v_xor_b32 v27, 128, v20\n" \
    "v_xor_b32 v28, 160, v20\n" \
    "v_xor_b32 v29, 192, v20\n" \
    "s_nop 0\n" \
    "ds_write_b128 v16, v[176:179] offset:0\n" \
    "ds_write_b128 v16, v[180:183] offset:1024\n" \
    "ds_write_b128 v16, v[184:187] offset:2048\n" \
    "ds_write_b128 v16, v[188:191] offset:3072\n" \
    "ds_read_b64_tr_b16 v[192:193], v20\n" \
    "ds_read_b64_tr_b16 v[194:195], v20 offset:1024\n" \
    "ds_read_b64_tr_b16 v[196:197], v24\n" \
    "ds_read_b64_tr_b16 v[198:199], v24 offset:1024\n" \
    "ds_read_b64_tr_b16 v[200:201], v25\n" \
    "ds_read_b64_tr_b16 v[202:203], v25 offset:1024\n" \
    "ds_read_b64_tr_b16 v[204:205], v26\n" \
    "ds_read_b64_tr_b16 v[206:207], v26 offset:1024\n" \
    "ds_read_b64_tr_b16 v[208:209], v27\n" \
    "ds_read_b64_tr_b16 v[210:211], v27 offset:1024\n" \
    "ds_read_b64_tr_b16 v[212:213], v28\n" \
    "ds_read_b64_tr_b16 v[214:215], v28 offset:1024\n" \
    "ds_read_b64_tr_b16 v[216:217], v29\n" \
    "ds_read_b64_tr_b16 v[218:219], v29 offset:1024\n" \
    "v_xor_b32 v24, 32, v21\n" \
    "v_xor_b32 v25, 64, v21\n" \
    "v_xor_b32 v26, 96, v21\n" \
    "v_xor_b32 v27, 128, v21\n" \
    "v_xor_b32 v28, 160, v21\n" \
    "v_xor_b32 v29, 192, v21\n" \
    "s_waitcnt lgkmcnt(0)\n" \
    "s_barrier\n" \
    "ds_read_b128 v[32:35], v17 offset:0\n" \
    "ds_read_b128 v[36:39], v17 offset:8192\n" \
    "ds_read_b128 v[40:43], v17 offset:16384\n" \
    "ds_read_b128 v[44:47], v17 offset:24576\n" \
    "ds_read_b64_tr_b16 v[220:221], v21\n" \
    "ds_read_b64_tr_b16 v[222:223], v21 offset:1024\n" \
    "ds_read_b64_tr_b16 v[224:225], v24\n" \
    "ds_read_b64_tr_b16 v[226:227], v24 offset:1024\n" \
    "ds_read_b64_tr_b16 v[228:229], v25\n" \
    "ds_read_b64_tr_b16 v[230:231], v25 offset:1024\n" \
    "ds_read_b64_tr_b16 v[232:233], v26\n" \
    "ds_read_b64_tr_b16 v[234:235], v26 offset:1024\n" \
    "ds_read_b64_tr_b16 v[236:237], v27\n" \
    "ds_read_b64_tr_b16 v[238:239], v27 offset:1024\n" \
    "s_waitcnt lgkmcnt(12)\n" \
    "v_pk_add_f32 v[32:33], v[32:33], v[36:37]\n" \
    "v_pk_add_f32 v[34:35], v[34:35], v[38:39]\n" \
    "v_mul_f32 v36, 0xbfb8aa3b, v32\n" \
    "v_mul_f32 v37, 0xbfb8aa3b, v33\n" \
    "v_mul_f32 v38, 0xbfb8aa3b, v34\n" \
    "v_mul_f32 v39, 0xbfb8aa3b, v35\n" \
    "v_exp_f32 v36, v36\n" \
    "v_exp_f32 v37, v37\n" \
    "v_exp_f32 v38, v38\n" \
    "v_exp_f32 v39, v39\n" \
    "s_waitcnt lgkmcnt(10)\n" \
    "ds_read_b64_tr_b16 v[240:241], v28\n" \
    "ds_read_b64_tr_b16 v[242:243], v28 offset:1024\n" \
    "ds_read_b64_tr_b16 v[244:245], v29\n" \
    "ds_read_b64_tr_b16 v[246:247], v29 offset:1024\n" \
    "v_pk_add_f32 v[40:41], v[40:41], v[44:45]\n" \
    "v_pk_add_f32 v[42:43], v[42:43], v[46:47]\n" \
    "v_add_f32 v36, 1.0, v36\n" \
    "v_add_f32 v37, 1.0, v37\n" \
    "v_add_f32 v38, 1.0, v38\n" \
    "v_add_f32 v39, 1.0, v39\n" \
    "v_rcp_f32 v36, v36\n" \
    "v_rcp_f32 v37, v37\n" \
    "v_rcp_f32 v38, v38\n" \
    "v_rcp_f32 v39, v39\n" \
    "v_pk_mul_f32 v[32:33], v[32:33], v[40:41]\n" \
    "v_pk_mul_f32 v[34:35], v[34:35], v[42:43]\n" \
    "v_pk_mul_f32 v[32:33], v[32:33], v[36:37]\n" \
    "v_pk_mul_f32 v[34:35], v[34:35], v[38:39]\n" \
    "v_cvt_pk_bf16_f32 v32, v32, v33\n" \
    "v_cvt_pk_bf16_f32 v33, v34, v35\n" \
    "ds_write_b64 v18, v[32:33]\n" \
    "s_waitcnt lgkmcnt(0)\n" \
    "v_mov_b32 v32, 1\n" \
    "s_mov_b64 exec, 1\n" \
    "ds_add_u32 v23, v32 offset:24\n" \
    "s_mov_b64 exec, -1\n" \
    "MOE_BS_%=:\n" \
    "ds_read_b32 v32, v23 offset:24\n" \
    "s_waitcnt lgkmcnt(0)\n" \
    "v_readfirstlane_b32 s61, v32\n" \
    "s_sub_u32 s60, s60, 1\n" \
    "s_cbranch_scc1 MOE_BT_%=\n" \
    "s_cmp_ge_u32 s61, 4\n" \
    "s_cbranch_scc0 MOE_BS_%=\n" \
    "MOE_BT_%=:\n" \
    "ds_read_b128 v[36:39], v19\n" \
    "ds_read_b128 v[40:43], v19 offset:64\n" \
    "s_waitcnt lgkmcnt(1)\n" \
    "v_mfma_f32_16x16x32_bf16 v[64:67], v[192:195], v[36:39], 0\n" \
    "v_mfma_f32_16x16x32_bf16 v[68:71], v[196:199], v[36:39], 0\n" \
    "v_mfma_f32_16x16x32_bf16 v[72:75], v[200:203], v[36:39], 0\n" \
    "v_mfma_f32_16x16x32_bf16 v[76:79], v[204:207], v[36:39], 0\n" \
    "v_mfma_f32_16x16x32_bf16 v[80:83], v[208:211], v[36:39], 0\n" \
    "v_mfma_f32_16x16x32_bf16 v[84:87], v[212:215], v[36:39], 0\n" \
    "v_mfma_f32_16x16x32_bf16 v[88:91], v[216:219], v[36:39], 0\n" \
    "s_waitcnt lgkmcnt(0)\n" \
    "v_mfma_f32_16x16x32_bf16 v[64:67], v[220:223], v[40:43], v[64:67]\n" \
    "v_mfma_f32_16x16x32_bf16 v[68:71], v[224:227], v[40:43], v[68:71]\n" \
    "v_mfma_f32_16x16x32_bf16 v[72:75], v[228:231], v[40:43], v[72:75]\n" \
    "v_mfma_f32_16x16x32_bf16 v[76:79], v[232:235], v[40:43], v[76:79]\n" \
    "v_mfma_f32_16x16x32_bf16 v[80:83], v[236:239], v[40:43], v[80:83]\n" \
    "v_mfma_f32_16x16x32_bf16 v[84:87], v[240:243], v[40:43], v[84:87]\n" \
    "v_mfma_f32_16x16x32_bf16 v[88:91], v[244:247], v[40:43], v[88:91]\n" \
    "s_nop 1\n" \
    "buffer_store_dwordx4 v[64:67], v22, %[ry], 0 offen offset:0 sc1\n" \
    "buffer_store_dwordx4 v[68:71], v22, %[ry], 0 offen offset:64 sc1\n" \
    "buffer_store_dwordx4 v[72:75], v22, %[ry], 0 offen offset:128 sc1\n" \
    "buffer_store_dwordx4 v[76:79], v22, %[ry], 0 offen offset:192 sc1\n" \
    "buffer_store_dwordx4 v[80:83], v22, %[ry], 0 offen offset:256 sc1\n" \
    "buffer_store_dwordx4 v[84:87], v22, %[ry], 0 offen offset:320 sc1\n" \
    "buffer_store_dwordx4 v[88:91], v22, %[ry], 0 offen offset:384 sc1\n" \
    "s_nop 1\n" \
    ""
#define MOE_FIN_CLOB "v23", "v24", "v25", "v26", "v27", "v28", "v29", "v30", "v32", "v33", "v34", "v35", "v36", "v37", "v38", "v39", "v40", "v41", "v42", "v43", "v44", "v45", "v46", "v47", "v48", "v49", "v50", "v51", "v52", "v53", "v54", "v55", "v56", "v57", "v58", "v59", "v60", "v61", "v62", "v63", "v144", "v145", "v146", "v147", "v148", "v149", "v150", "v151", "v152", "v153", "v154", "v155", "v156", "v157", "v158", "v159", "v160", "v161", "v162", "v163", "v164", "v165", "v166", "v167", "v168", "v169", "v170", "v171", "v172", "v173", "v174", "v175", "v176", "v177", "v178", "v179", "v180", "v181", "v182", "v183", "v184", "v185", "v186", "v187", "v188", "v189", "v190", "v191", "v192", "v193", "v194", "v195", "v196", "v197", "v198", "v199", "v200", "v201", "v202", "v203", "v204", "v205", "v206", "v207", "v208", "v209", "v210", "v211", "v212", "v213", "v214", "v215", "v216", "v217", "v218", "v219", "v220", "v221", "v222", "v223", "v224", "v225", "v226", "v227", "v228", "v229", "v230", "v231", "v232", "v233", "v234", "v235", "v236", "v237", "v238", "v239", "v240", "v241", "v242", "v243", "v244", "v245", "v246", "v247", "s60", "s61"

template <bool B> struct BoolC { static constexpr bool value = B; };
DI rsrc_t mkrs(const void* p, unsigned bytes) { return __builtin_amdgcn_make_buffer_rsrc((void*)p, (short)0, (int)bytes, 0x00020000); }
DI void dma1(unsigned m0v, unsigned vo, rsrc_t rs, unsigned so) {
    asm volatile("s_mov_b32 m0, %0\n\ts_nop 0\n\tbuffer_load_dwordx4 %1, %2, %3 offen lds" ::"s"(m0v), "v"(vo), "s"(rs), "s"(so) : "memory");
}
DI void dma1c(unsigned m0v, unsigned vo, rsrc_t rs, unsigned so) {
    asm volatile("s_mov_b32 m0, %0\n\ts_nop 0\n\tbuffer_load_dwordx4 %1, %2, %3 offen sc1 lds" ::"s"(m0v), "v"(vo), "s"(rs), "s"(so) : "memory");
}
DI void dma2(unsigned m0v, unsigned v0, unsigned v1, rsrc_t rs, unsigned so) {
    asm volatile("s_mov_b32 m0, %0\n\ts_nop 0\n\tbuffer_load_dwordx4 %1, %3, %4 offen lds\n\tbuffer_load_dwordx4 %2, %3, %4 offen offset:1024 lds"
                 ::"s"(m0v), "v"(v0), "v"(v1), "s"(rs), "s"(so) : "memory");
}
DI void dma4(unsigned m0v, unsigned v0, unsigned v1, unsigned v2, unsigned v3, rsrc_t rs, unsigned so) {
    asm volatile("s_mov_b32 m0, %0\n\ts_nop 0\n\tbuffer_load_dwordx4 %1, %5, %6 offen lds\n\tbuffer_load_dwordx4 %2, %5, %6 offen offset:1024 lds\n\t"
                 "buffer_load_dwordx4 %3, %5, %6 offen offset:2048 lds\n\tbuffer_load_dwordx4 %4, %5, %6 offen offset:3072 lds"
                 ::"s"(m0v), "v"(v0), "v"(v1), "v"(v2), "v"(v3), "s"(rs), "s"(so) : "memory");
}
#define WAITV(n) asm volatile("s_waitcnt vmcnt(" #n ")" ::: "memory")
#define BAR() asm volatile("s_waitcnt lgkmcnt(0)\n\ts_barrier" ::: "memory")
#define RLX __ATOMIC_RELAXED, __HIP_MEMORY_SCOPE_AGENT
template <class T> DI T ldsr(unsigned a) { return *(const LAS T*)a; }
template <class T> DI void ldsw(unsigned a, T v) { *(LAS T*)a = v; }
DI s16x4 trr(unsigned a) { return __builtin_amdgcn_ds_read_tr16_b64_v4i16((LAS s16x4*)a); }
DI bf16x8 tr8(unsigned a0, unsigned a1) { s16x4 lo = trr(a0), hi = trr(a1); return __builtin_bit_cast(bf16x8, __builtin_shufflevector(lo, hi, 0, 1, 2, 3, 4, 5, 6, 7)); }
DI unsigned pk(float lo, float hi) { f32x2 v = {lo, hi}; return __builtin_bit_cast(unsigned, __builtin_convertvector(v, bf16x2)); }
DI float bflo(unsigned u) { return __uint_as_float(u << 16); }
DI float bfhi(unsigned u) { return __uint_as_float(u & 0xffff0000u); }
DI f32x16 mf32(bf16x8 a, bf16x8 b, f32x16 c) { return __builtin_amdgcn_mfma_f32_32x32x16_bf16(a, b, c, 0, 0, 0); }
DI f32x4 mf16(bf16x8 a, bf16x8 b, f32x4 c) { return __builtin_amdgcn_mfma_f32_16x16x32_bf16(a, b, c, 0, 0, 0); }
DI float sigm(float v) { return __builtin_amdgcn_rcpf(1.f + __builtin_amdgcn_exp2f(-1.4426950408889634f * v)); }
template <int SH, int SLP = 1> DI u64 pollw(const char* p, u64 want) {
    u64 v;
    for (unsigned n = 0;; ++n) {
        v = __hip_atomic_load((const GAS u64*)p, RLX);
        if (__all((v >> SH) == want) || n > SPIN_CAP) break;
        __builtin_amdgcn_s_sleep(SLP);
    }
    asm volatile("" ::: "memory");
    return v;
}
DI void stflag(char* p, u64 v) { __hip_atomic_store((GAS u64*)p, v, RLX); }
DI void top8(unsigned a, unsigned b, char* dst, u64 tag) {
    unsigned thr, bit, tn, c; u64 m;
    asm volatile("s_mov_b32 %0, 0\n\ts_mov_b32 %1, 0x4000000\n"
                 "T8L_%=:\n\ts_or_b32 %2, %0, %1\n\tv_cmp_le_u32 %4, %2, %5\n\tv_cmp_le_u32 vcc, %2, %6\n\t"
                 "s_bcnt1_i32_b64 %3, %4\n\ts_bcnt1_i32_b64 vcc_lo, vcc\n\ts_add_u32 %3, %3, vcc_lo\n\t"
                 "s_cmp_ge_u32 %3, 8\n\ts_cselect_b32 %0, %2, %0\n\ts_cmp_eq_u32 %3, 8\n\ts_cbranch_scc1 T8D_%=\n\t"
                 "s_lshr_b32 %1, %1, 1\n\ts_cbranch_scc1 T8L_%=\n"
                 "T8D_%=:"
                 : "=&s"(thr), "=&s"(bit), "=&s"(tn), "=&s"(c), "=&s"(m) : "v"(a), "v"(b) : "vcc", "scc");
    const u64 ma = __ballot(a >= thr), mb = __ballot(b >= thr);
    const unsigned sa = __builtin_amdgcn_mbcnt_hi((unsigned)(ma >> 32), __builtin_amdgcn_mbcnt_lo((unsigned)ma, 0u));
    const unsigned sb = __builtin_amdgcn_mbcnt_hi((unsigned)(mb >> 32), __builtin_amdgcn_mbcnt_lo((unsigned)mb, (unsigned)__popcll(ma)));
    if (a >= thr && sa < 8) stflag(dst + sa * 8, (tag << 27) | a);
    if (b >= thr && sb < 8) stflag(dst + sb * 8, (tag << 27) | b);
}
#define STAMP(k)
#define STAMPC(k)
#define DUMP()

#define STAMPM(k)
DI unsigned kvflag(int l, int idx) { return WS_KVFLAG + ((l >> 2) * 16 + (((7 * idx + ((l & 1) ? 6 : 0)) >> 4) + ((l & 2) ? 7 : 0))) * 128; }
DI int etok(int wv, int l, int ct) { return ct < 3 ? 4 * wv + (l >> 4) : 8 * wv + (l >> 3); }
DI int ech(int l, int ct) { return ct < 3 ? l & 15 : l & 7; }
DI unsigned rowoff(int wv, int l, int t, int tt, int ct) { return (t * 128 + tt * 32 + etok(wv, l, ct)) * 896 + ct * 256 + ech(l, ct) * 16; }
DI void epilogue(char* out, int wv, int l, int t, int tt, int ct, u32x4 xres) {
    const int tok = etok(wv, l, ct), ch = ech(l, ct);
    const u32x4 a = ldsr<u32x4>(L_T + tok * (ct < 3 ? 272 : 144) + ch * 16);
    const float g = ldsr<float>(L_GS + tok * 4), g1 = 1.f - g;
    const int e = ldsr<int>(L_MAP + tok * 4);
    f32x4 y0 = {0.f, 0.f, 0.f, 0.f}, y1 = y0;
    if (e >= 0) { y0 = ldsr<f32x4>(L_YS + e * 512 + ch * 32); y1 = ldsr<f32x4>(L_YS + e * 512 + ch * 32 + 16); }
    u32x4 o;
    o[0] = pk(g * bflo(a[0]) + g1 * (bflo(xres[0]) + y0[0]), g * bfhi(a[0]) + g1 * (bfhi(xres[0]) + y0[1]));
    o[1] = pk(g * bflo(a[1]) + g1 * (bflo(xres[1]) + y0[2]), g * bfhi(a[1]) + g1 * (bfhi(xres[1]) + y0[3]));
    o[2] = pk(g * bflo(a[2]) + g1 * (bflo(xres[2]) + y1[0]), g * bfhi(a[2]) + g1 * (bfhi(xres[2]) + y1[1]));
    o[3] = pk(g * bflo(a[3]) + g1 * (bflo(xres[3]) + y1[2]), g * bfhi(a[3]) + g1 * (bfhi(xres[3]) + y1[3]));
    __builtin_amdgcn_raw_buffer_store_b128(o, mkrs(out, 2048 * 896), rowoff(wv, l, t, tt, ct), 0, 16);
}

__global__ __launch_bounds__(512, 2) void fused_kernel(const char* __restrict__ x, const char* __restrict__ wqkv, const char* __restrict__ wo,
                                                       const char* __restrict__ wr, const char* __restrict__ wgate, const char* __restrict__ wup,
                                                       const char* __restrict__ wdown, const char* __restrict__ gw, char* __restrict__ out, char* ws) {
    const int b = blockIdx.x, xcd = b & 7, i = b >> 3;
    u32x4 xv; u32x2 wvv; unsigned wqv, tid = threadIdx.x;
    asm volatile(ENTRY_ASM : "=&{v[4:7]}"(xv), "=&{v[8:9]}"(wvv), "=&{v10}"(wqv), "+{v0}"(tid)
                 : [ka] "s"(__builtin_amdgcn_kernarg_segment_ptr()), [b] "s"(b)
                 : "v1", "v2", "v3", "s60", "s61", "s62", "s63", "s64", "s65", "s66", "s67", "s68", "s69", "s70", "s71", "s72", "s73", "s74", "s75", "s76", "s77", "s78", "s79", "vcc", "scc", "memory");
    const int t = (xcd & 3) * 4 + (i & 3), idx = (i >> 2) * 2 + (xcd >> 2), rot0 = (i >> 2) + 2 * (i & 3);
    const int wv = __builtin_amdgcn_readfirstlane(tid >> 6), l = tid & 63;
    const int h2 = l >> 5, r = l & 31, r8 = l >> 3, c8 = l & 7;
    const int tq = (l & 15) >> 2, tp = l & 3, blk = (l >> 4) & 1;
    const bool spare = idx >= 14;
    const int mid = idx == 15 ? t : 16 + t;
    const bool moe = spare && mid < 28;
    const int kk = idx >> 1, tl0 = kk >= 3 && kk < 6, tt = 2 * (idx & 1) + tl0, ct = kk < 6 ? kk - 3 * tl0 : 3;
    const rsrc_t rx = mkrs(x, 2048 * 896);
    const int rot = rot0 >= 7 ? rot0 - 7 : rot0;
    const unsigned xr = L_XR + (wv & 3) * 16384;
#define CH(p) ((p) + rot >= 7 ? (p) + rot - 7 : (p) + rot)
    if (wv < 4) {
        int l = tid & 63; asm volatile("" : "+v"(l));
        const int h2 = l >> 5, r = l & 31, tq = (l & 15) >> 2, tp = l & 3, blk = (l >> 4) & 1;
        const rsrc_t rkv = mkrs(ws + WS_KV, 4u << 20);
        u32x4 pd; unsigned po;
        {
            const int jj = idx < 7 ? idx : idx - 7, hh = (16 * jj) / 7 + (l >> 4), li = l & 15;
            const unsigned one = (idx < 7 || (li & 3) == 0) ? 0x3f803f80u : 0u, w1 = idx < 7 ? 0x00003f80u : one;
            const unsigned off = idx < 7 ? 7 * 256 + li * 16 : 2048 + (li >> 3) * 1024 + ((li >> 2) & 1) * 512 + (28 + (li & 3)) * 16;
            pd = (u32x4){one, w1, one, w1};
            po = (!spare && hh < (16 * (jj + 1)) / 7) ? WS_KV + (t * 4 + wv) * 4096 + hh * 262144 + off : 0x7ff00000u;
        }
        STAMP(1);
        u32x8 so;
#pragma unroll
        for (int q = 0; q < 8; ++q) { const unsigned G = 16 * idx + 2 * q + h2, hh = (G * 147) >> 10; so[q] = hh * 262144 + (G - 7 * hh) * 256 + r * 8; }
        if (idx >= 7) {
#pragma unroll
            for (int n = 0; n < 2; ++n) { const unsigned C = 64 * (idx - 7) + 32 * n + r, hh = (C * 2341) >> 16; so[n] = hh * 262144 + 2048 + h2 * 512 + (C - 28 * hh) * 16; }
        }
        u32x4 xad, pc;
#pragma unroll
        for (int s = 0; s < 4; ++s) xad[s] = xr + r * 128 + (((((r >> 1) & 7) ^ h2) << 4) ^ (32 * s));
        pc[0] = L_WKV + (8 * h2 + tq) * 128 + (((blk * 2 + (tp >> 1)) ^ ((tq >> 1) << 2)) << 4) + (tp & 1) * 8;
        pc[1] = L_WQ + (8 * h2 + tq) * 64 + ((((idx * 56) & 15) + blk * 32 + tp * 8) & 63);
        pc[2] = L_KEY + (wv * 32 + r) * 4;
        pc[3] = kvflag(l, idx);
        u32x8 tc;
        tc[0] = ct < 3 ? L_WO + (4 * h2 + tq) * 256 + (((wv * 4 + blk * 2 + (tp >> 1)) ^ (tq << 2)) << 4) + (tp & 1) * 8
                       : L_XR + (4 * h2 + tq) * 128 + ((((wv & 1) * 4 + blk * 2 + (tp >> 1)) ^ ((tq >> 1) << 2)) << 4) + (tp & 1) * 8;
        tc[1] = L_T + (ct < 3 ? r * 272 + wv * 64 : ((wv >> 1) * 32 + r) * 144 + (wv & 1) * 64) + h2 * 8;
        tc[2] = rowoff(wv, l, t, tt, ct);
        tc[3] = L_T + etok(wv, l, ct) * (ct < 3 ? 272 : 144) + ech(l, ct) * 16;
        tc[4] = L_GS + etok(wv, l, ct) * 4;
        tc[5] = h2 * (ct < 3 ? 1024 : 512); tc[6] = L_YS + ech(l, ct) * 32; tc[7] = ((ct == 3 && wv >= 2) ? 0 : L_OF) + l * 16;
        int lv = l;
        asm volatile(MAIN_ASM
            :   "+{v0}"(lv), "+{v[4:7]}"(pd), "+{v12}"(po), "+{v[16:19]}"(xad), "+{v[24:27]}"(pc), "+{v[32:39]}"(so), "+{v[248:255]}"(tc)
            : [rx] "s"(rx), [rkv] "s"(rkv), [ratt] "s"(mkrs(ws + WS_KV + idx * 262144 + wv * 65536, 65536)),
              [ro] "s"(mkrs(ws + WS_O, 2u << 20)), [rout] "s"(mkrs(out, 2048 * 896)), [ws] "s"(ws), [rws] "s"(mkrs(ws, 8u << 20)),
              [t] "s"(t), [idx] "s"(idx), [wv] "s"(wv), [rot] "s"(rot), [tt] "s"(tt), [ct] "s"(ct)
            : MAIN_CLOB, "vcc", "scc", "memory");
    } else {
        const int hw = wv - 4;
        const rsrc_t rg = mkrs((hw >> 1) ? wup : wgate, 448 * 3584);
        const unsigned gph = (r8 >> 1) & 1;
        const unsigned gv0 = (224 * (hw & 1) + r8) * 3584 + mid * 128 + ((c8 ^ (gph << 1)) << 4);
        const unsigned gv1 = (224 * (hw & 1) + r8 + 8) * 3584 + mid * 128 + ((c8 ^ ((gph | 2) << 1)) << 4) - 1024;
        const unsigned areaA = hw * 14336, areaB = L_XR + hw * 8192;
        u64 tag;
        {
            u32x8 a;
            a[0] = wvv[0]; a[1] = wvv[1]; a[2] = wqv;
            const int pa = (l >> 4) + rot, pb = (l < 48 ? (l >> 4) : 0) + 4 + rot;
            const unsigned ka = 64 * (pa >= 7 ? pa - 7 : pa) + 16 * hw + (l & 15), kb = 64 * (pb >= 7 ? pb - 7 : pb) + 16 * hw + (l & 15);
            a[3] = L_KEY + 1280 + 2 * ka; a[4] = L_KEY + 1280 + 2 * kb;
            a[5] = L_WQ + ka * 64 + ((((idx * 56) & 15) + 56) & 63); a[6] = L_WQ + kb * 64 + ((((idx * 56) & 15) + 56) & 63);
            a[7] = 0;
            asm volatile(LOAD_ASM
                : [tag] "=&s"(tag)
                : "{v[4:7]}"(xv), "{v[8:15]}"(a), "{v21}"(moe ? gv0 : 0x7ff00000u), "{v30}"(moe ? gv1 : 0x7ff00000u), [rg] "s"(rg), [rx] "s"(rx), [rw] "s"(mkrs(wqkv, 448 * 2688)), [xr] "s"(xr), [hw] "s"(hw), [rot] "s"(rot), [idx] "s"(idx)
                : LOAD_CLOB, "vcc", "scc", "memory");
        }
        const rsrc_t ry = mkrs(ws + WS_YP, 28 * 14336);
        const int g4 = l >> 4;
        u32x4 xres; unsigned key, hit; int tl;
        auto mid_part = [&](auto moe_c) __attribute__((always_inline)) {
        constexpr bool MOE = decltype(moe_c)::value;
        STAMP(4);
        u32x16 gf0, gf1, gf2, gf3, gf4;
        u32x4 gaA, gaB;
        {
            const unsigned gad = (8 * g4 + tq) * 128 + (tp >> 1) * 16 + (tp & 1) * 8, gx = ((tq >> 1) | ((g4 & 1) << 1)) << 5;
#pragma unroll
            for (int c = 0; c < 4; ++c) { gaA[c] = areaA + gad + ((c << 5) ^ gx); gaB[c] = areaB + gad + ((c << 5) ^ gx); }
        }
#define GFRAGS() asm volatile("s_waitcnt vmcnt(0)\n" MOE_G1_ASM : "=&{v[64:79]}"(gf0), "=&{v[80:95]}"(gf1), "=&{v[96:111]}"(gf2) : "{v[8:11]}"(gaA) : "memory")
        if (MOE) GFRAGS();
        if (idx == 14) {
            BAR();
            if (hw == 0) top8(ldsr<unsigned>(L_KEY + l * 4), ldsr<unsigned>(L_KEY + 256 + l * 4), ws + WS_CAND + t * 128, tag);
        }
        if (!spare && hw == 1) pollw<0>(ws + kvflag(l, idx), tag);
        BAR();
        STAMP(5);
        if (!MOE && idx == 14 && t == 15 && hw == 2) {
            const char* cp = ws + WS_CAND + (l >> 3) * 128 + (l & 7) * 8;
            u64 ka, kb;
            for (unsigned n = 0;; ++n) {
                ka = __hip_atomic_load((const GAS u64*)cp, RLX); kb = __hip_atomic_load((const GAS u64*)(cp + 1024), RLX);
                if (__all((ka >> 27) == (tag & TAG37) && (kb >> 27) == (tag & TAG37)) || n > SPIN_CAP) break;
            }
            top8((unsigned)ka & 0x7ffffffu, (unsigned)kb & 0x7ffffffu, ws + WS_SEL, tag);
        }
        __builtin_amdgcn_s_sleep(MOE ? T_SLEEP2 : T_SLEEP0);
        {
            const u32x4 em = (u32x4)(EMPTY);
            const rsrc_t rs = mkrs(ws, 8u << 20);
#pragma unroll
            for (int n = 0; n < 2; ++n) __builtin_amdgcn_raw_buffer_store_b128(em, rs, WS_O + ((t * 4 + hw) * 16 + idx) * 2048 + n * 1024 + l * 16, 0, 16);
            if (MOE) {
#pragma unroll
                for (int n = 0; n < 4; ++n) __builtin_amdgcn_raw_buffer_store_b128(em, rs, (n < 3 || l < 32) ? WS_YP + mid * 14336 + hw * 3584 + n * 1024 + l * 16 : 0x7ff00000u, 0, 16);
            }
        }
#define CLEARED() do { unsigned old_ = 0; if (l == 0) old_ = __hip_atomic_fetch_add((LAS unsigned*)(L_CTL + 20), 1u, __ATOMIC_RELAXED, __HIP_MEMORY_SCOPE_WORKGROUP); \
            if (old_ == 3) stflag(ws + WS_OFLAG + (t * 16 + idx) * 128, tag); } while (0)
        STAMP(3);
        const rsrc_t rwo = mkrs(wo, 448 * 896);
#define SELECTED() do { key = (unsigned)pollw<27, 8>(ws + WS_SEL + (l & 7) * 8, tag & TAG37) & 0x7ffffffu; \
            tl = (2047 - (int)(key & 2047u)) - (t * 128 + tt * 32);     \
            hit = (unsigned)__ballot(tl >= 0 && tl < (ct < 3 ? 32 : 64)) & 0xffu; } while (0)
        if (MOE) {
            const rsrc_t rd = mkrs(wdown, 1792 * 896);
            const unsigned dbase = (64 * mid + g4) * 896 + hw * 224;
            const unsigned dv0 = dbase + (((l & 15) ^ (g4 << 1)) << 4), dv1 = dbase + (((l & 15) ^ ((g4 | 4) << 1)) << 4);
#define WDN(n) do { const unsigned v_ = ((n) & 1) ? dv1 : dv0; dma2((n) < 7 ? areaA + (n) * 2048 : L_ALT + hw * 2048, v_, v_ + 3584 - 1024, rd, (n) * 8 * 896); } while (0)
#pragma unroll 1
            for (int n = 6; n < 10; ++n) { dma2(areaB + (n - 6) * 2048, gv0, gv1, rg, n * 16 * 3584); WAITX(T_FLY); }
            CLEARED();
#pragma unroll 1
            for (int n = 0; n < 2; ++n) { WDN(n); WAITX(T_FLY); }
            asm volatile(MOE_G2_ASM : "=&{v[112:127]}"(gf3), "=&{v[128:143]}"(gf4) : "{v[12:15]}"(gaB) : "memory");
#pragma unroll 1
            for (int n = 10; n < 14; ++n) { dma2(areaB + (n - 10) * 2048, gv0, gv1, rg, n * 16 * 3584); WAITX(T_FLY); }
#pragma unroll 1
            for (int n = 2; n < 8; ++n) { WDN(n); WAITX(T_FLY); }
            STAMP(6);
            SELECTED();
            STAMP(13);
#pragma unroll
            for (int u = 0; u < 2; ++u) {
                const unsigned tok = 2047 - ((unsigned)__builtin_amdgcn_readlane((int)key, 2 * hw + u) & 2047u);
                asm volatile("s_mov_b32 m0, %0\n\ts_mov_b32 exec_hi, 0xffffff\n\tbuffer_load_dwordx4 %1, %2, %3 offen lds\n\ts_mov_b32 exec_hi, -1"
                             ::"s"(L_XS + (2 * hw + u) * 912), "v"(l * 16), "s"(rx), "s"(tok * 896) : "memory");
            }
            const unsigned d0 = (8 * g4 + tq) * 256 + (tp >> 1) * 16 + (tp & 1) * 8 + ((tq | ((g4 & 1) << 2)) << 5);
            u32x4 ea = {areaB + l * 16, L_XR + hw * 1024 + l * 16, L_EX2 + (l & 15) * 144 + (16 * hw + 4 * g4) * 2, L_EX2 + (l & 15) * 144 + 16 * g4};
            u32x2 da = {areaA + d0, g4 == 3 ? L_ALT + hw * 2048 - 24 * 256 + d0 : areaA + d0 + 8192};
            const unsigned yo = (l & 15) < 8 ? mid * 14336 + (l & 15) * 1792 + (112 * hw + 4 * g4) * 4 : 0x7ff00000u;
            WAITV(0);
            asm volatile(MOE_FIN_ASM
                : "+{v[64:79]}"(gf0), "+{v[80:95]}"(gf1), "+{v[96:111]}"(gf2), "+{v[112:127]}"(gf3), "+{v[128:143]}"(gf4)
                : "{v[12:15]}"(gaB), "{v[16:19]}"(ea), "{v[20:21]}"(da), "{v22}"(yo), "{v31}"(L_XS + (l & 7) * 912 + 448 * (hw & 1) + 16 * g4), [ry] "s"(ry)
                : MOE_FIN_CLOB, "scc", "memory");
            STAMP(8);
        } else if (spare) {
            WAITV(0); CLEARED();
            BAR(); BAR();
        } else {
            xres = __builtin_amdgcn_raw_buffer_load_b128(rx, rowoff(wv, l, t, tt, ct), 0, 0);
            if (ct < 3) {
                const unsigned v0 = g4 * 896 + ct * 256 + (((l & 15) ^ (g4 << 2)) << 4);
#pragma unroll 1
                for (int n = 0; n < 14; ++n) { const int p = 28 * hw + 2 * n; dma2(L_WO + p * 1024, v0, v0 + 3584 - 1024, rwo, p * 3584); WAITX(T_FLY); if (n == 1) CLEARED(); }
            } else {
                const unsigned v0 = r8 * 896 + 768 + ((c8 ^ (((r8 >> 1) & 1) << 2)) << 4);
#pragma unroll 1
                for (int n = 0; n < 7; ++n) { const int p = 14 * hw + 2 * n; dma2(L_XR + p * 1024, v0, v0 + 7168 - 1024, rwo, p * 7168); WAITX(T_FLY); if (n == 1) CLEARED(); }
            }
            STAMP(6);
            SELECTED();
            STAMP(13);
        }
        };
        if (moe) mid_part(BoolC<true>{}); else mid_part(BoolC<false>{});
        if (spare) { STAMP(12); DUMP(); return; }
        {
            const bool all = (hw < 2 && ((hit >> hw) & 0x55u)) || (b == 0 && hw == 0);
            const int k = (l - 16) & 31;
            pollw<0, 8>(ws + WS_OFLAG + ((l < 16 || !all) ? t * 16 + (l & 15) : (k >> 1) * 16 + 14 + (k & 1)) * 128, tag);
        }
        WAITV(0);
        STAMP(8);
        BAR();
        BAR();
        BAR();
        STAMP(9);
        if (ct < 3) __builtin_amdgcn_s_sleep(T_SLEEP1); else __builtin_amdgcn_s_sleep(T_SLEEP3);
        STAMP(7);
        {
            const rsrc_t ro = mkrs(ws + WS_O + ((t * 4 + tt) * 16 + hw) * 2048, 29 * 2048);
            const unsigned vo = l * 16;
            u32x4 o[16];
#define OLOAD(k) asm volatile("buffer_load_dwordx4 %0, %2, %3, %4 offen sc1\n\tbuffer_load_dwordx4 %1, %2, %3, %4 offen offset:1024 sc1" \
                              : "=&v"(o[2 * (k)]), "=&v"(o[2 * (k) + 1]) : "v"(vo), "s"(ro), "s"((k) * 8192) : "memory")
#define OWAIT(k, n) asm volatile("s_waitcnt vmcnt(%2)" : "+v"(o[2 * (k)]), "+v"(o[2 * (k) + 1]) : "i"(n) : "memory")
#define OBAD(k) ({ const u32x4 m_ = __builtin_elementwise_max(o[2 * (k)], o[2 * (k) + 1]); __any(max(max(m_[0], m_[1]), max(m_[2], m_[3])) == EMPTY); })
            const unsigned oa0 = L_OF + hw * 2048 + vo, oa1 = hw * 2048 + vo, rdy = L_RDY;
#define OPUT(k) asm volatile("ds_write_b128 %0, %1 offset:%5\n\tds_write_b128 %0, %2 offset:%5+1024\n\ts_mov_b64 exec, 1\n\tds_or_b32 %3, %4\n\ts_mov_b64 exec, -1" \
                             ::"v"((k) < 4 ? oa0 : oa1), "v"(o[2 * (k)]), "v"(o[2 * (k) + 1]), "v"(rdy), "v"((1u << (4 * (k))) << hw), "i"(((k) & 3) * 8192) : "memory")
#define OSTEP(k, n) OWAIT(k, n); if (OBAD(k)) bad |= 1u << (k); else OPUT(k);
#define MAPW() if (hw == 1) { int e = -1; _Pragma("unroll") for (int k = 0; k < 8; ++k) e = __builtin_amdgcn_readlane(tl, k) == l ? k : e; ldsw<int>(L_MAP + l * 4, e); }
            unsigned bad = 0;
            if (ct < 3) {
                OLOAD(0); OLOAD(1); OLOAD(2); OLOAD(3);
                MAPW()
                OSTEP(0, 6) OSTEP(1, 4) OSTEP(2, 2) OSTEP(3, 0)
            } else {
                OLOAD(0); OLOAD(4); OLOAD(1); OLOAD(5); OLOAD(2); OLOAD(6); OLOAD(3); OLOAD(7);
                MAPW()
                OSTEP(0, 14) OSTEP(4, 12) OSTEP(1, 10) OSTEP(5, 8) OSTEP(2, 6) OSTEP(6, 4) OSTEP(3, 2) OSTEP(7, 0)
            }
            STAMP(15);
#pragma unroll 1
            for (unsigned n = 0; bad; ++n) {
                const unsigned rest = bad & (bad - 1);
                const int k0 = __builtin_ctz(bad), k1 = rest ? __builtin_ctz(rest) : k0;
                u32x4 p[4];
                asm volatile("buffer_load_dwordx4 %0, %4, %6, %7 offen sc1\n\tbuffer_load_dwordx4 %1, %4, %6, %7 offen offset:1024 sc1\n\t"
                             "buffer_load_dwordx4 %2, %5, %6, %8 offen sc1\n\tbuffer_load_dwordx4 %3, %5, %6, %8 offen offset:1024 sc1\n\ts_waitcnt vmcnt(0)"
                             : "=&v"(p[0]), "=&v"(p[1]), "=&v"(p[2]), "=&v"(p[3]) : "v"(vo), "v"(rest ? vo : 0x7ff00000u), "s"(ro), "s"(k0 * 8192), "s"(k1 * 8192) : "memory");
#pragma unroll
                for (int u = 0; u < 2; ++u) {
                    const int k = u ? k1 : k0;
                    const u32x4 m = __builtin_elementwise_max(p[2 * u], p[2 * u + 1]);
                    if ((u && !rest) || (__any(max(max(m[0], m[1]), max(m[2], m[3])) == EMPTY) && n <= SPIN_CAP)) continue;
                    asm volatile("ds_write_b128 %0, %1\n\tds_write_b128 %0, %2 offset:1024\n\ts_mov_b64 exec, 1\n\tds_or_b32 %3, %4\n\ts_mov_b64 exec, -1"
                                 ::"v"((k < 4 ? oa0 : oa1) + (k & 3) * 8192), "v"(p[2 * u]), "v"(p[2 * u + 1]), "v"(rdy), "v"((1u << (4 * k)) << hw) : "memory");
                    bad &= ~(1u << k);
                }
            }
        }
        STAMP(10);
#pragma unroll 1
        for (unsigned mine = hw < 2 ? (hit >> hw) & 0x55u : 0u; mine; mine &= mine - 1) {
            const int e = hw + __builtin_ctz(mine);
            const unsigned yo = h2 * 14336 + e * 1792 + (ct * 128 + ((ct < 3 || r < 16) ? r : 0) * 4) * 4;
            const unsigned ko = (unsigned)__builtin_amdgcn_readlane((int)key, e) >> 11;
            const float rs = sigm(bflo(ko ^ ((ko & 0x8000u) ? 0x8000u : 0xffffu)));
            u32x4 v[14];
            f32x4 y;
            unsigned pend = 0x3fff;
#pragma unroll 1
            for (unsigned n = 0;; ++n) {
#pragma unroll
                for (int p = 0; p < 14; ++p)
                    if ((pend >> p) & 1) asm volatile("buffer_load_dwordx4 %0, %1, %2, %3 offen sc1" : "+v"(v[p]) : "v"(yo), "s"(ry), "s"(p * 2 * 14336) : "memory");
                asm volatile("s_waitcnt vmcnt(0)" : "+v"(v[0]), "+v"(v[1]), "+v"(v[2]), "+v"(v[3]), "+v"(v[4]), "+v"(v[5]), "+v"(v[6]), "+v"(v[7]),
                             "+v"(v[8]), "+v"(v[9]), "+v"(v[10]), "+v"(v[11]), "+v"(v[12]), "+v"(v[13])::"memory");
                y = __builtin_bit_cast(f32x4, v[0]);
#pragma unroll
                for (int p = 1; p < 14; ++p) y += __builtin_bit_cast(f32x4, v[p]);
                const float c = (y[0] + y[1]) + (y[2] + y[3]);
                if (!__any(c != c) || n > SPIN_CAP) break;
#pragma unroll
                for (int p = 0; p < 14; ++p) pend = __any(max(max(v[p][0], v[p][1]), max(v[p][2], v[p][3])) == EMPTY) ? pend : pend & ~(1u << p);
                if (!pend) break;
            }
#pragma unroll
            for (int k = 0; k < 4; ++k) {
                auto a = __builtin_amdgcn_permlane32_swap(__float_as_uint(y[k]), __float_as_uint(y[k]), false, false);
                y[k] = (__uint_as_float(a[0]) + __uint_as_float(a[1])) * rs;
            }
            if (l < 32) ldsw<f32x4>(L_YS + e * 512 + l * 16, y);
        }
        STAMP(14);
        asm volatile("s_waitcnt lgkmcnt(0)\n\ts_barrier\nfk_end_b:" ::: "memory");
        STAMP(11);
        if (b == 0 && hw == 0 && l == 0) stflag(ws + WS_EPOCH, tag);
        epilogue(out, wv, l, t, tt, ct, xres);
        STAMP(12); DUMP();
    }
}

extern "C" void kernel_launch(void* const* d_in, const int* in_sizes, int n_in, void* d_out, int out_size, void* d_ws, size_t ws_size,
                              hipStream_t stream) {
    (void)in_sizes; (void)n_in; (void)out_size; (void)ws_size;
    (void)hipFuncSetAttribute((const void*)fused_kernel, hipFuncAttributeMaxDynamicSharedMemorySize, LDS_BYTES);
    fused_kernel<<<256, 512, LDS_BYTES, stream>>>((const char*)d_in[0], (const char*)d_in[1], (const char*)d_in[2], (const char*)d_in[4],
                                                  (const char*)d_in[5], (const char*)d_in[6], (const char*)d_in[7], (const char*)d_in[8],
                                                  (char*)d_out, (char*)d_ws);
}
```
